```python
import torch

def _softmax(x, axis=-1):
    return torch.softmax(x, dim=axis)
def _gelu(x):
    return torch.nn.functional.gelu(x, approximate="tanh")
def _silu(x):
    return torch.nn.functional.silu(x)
def _sigmoid(x):
    return torch.sigmoid(x)
def _layernorm(x, axis=-1, eps=1e-5):
    m = x.mean(dim=axis, keepdim=True)
    v = x.var(dim=axis, keepdim=True, unbiased=False)
    return (x - m) / torch.sqrt(v + eps)
def _scatter_add(tgt, idx, src, axis=0):
    return tgt.clone().index_add_(axis, idx, src)
def _segment_sum(src, seg, n):
    out = torch.zeros((n,) + src.shape[1:], dtype=src.dtype)
    return out.index_add_(0, seg, src)
def _pad(x, pw):
    flat = tuple(v for pair in reversed(pw) for v in pair)
    return torch.nn.functional.pad(x, flat)

kernel_name = 'gated_mix__mha_block__ssm_conv_h3__prompt'

def setup_inputs():

    _w = (lambda a: (lambda t: t.to(torch.bfloat16) if t.is_floating_point() else t)(torch.as_tensor(a)))
    def _A_setup():
        B, S, D, H = 1, 2048, 576, 32
        return {
            "x": _w((lambda *s: torch.randn(*s, dtype=torch.bfloat16) * 0.05)(B, S, D)),
            "wqkv": _w((lambda *s: torch.randn(*s, dtype=torch.bfloat16) * 0.8)(D, 3 * D)),
            "wo": _w((lambda *s: torch.randn(*s, dtype=torch.bfloat16) * 0.05)(D, D)),
        }
    _A_in = _A_setup()

    def _B_setup():
        B, S, D = 1, 2048, 576
        ks = 4
        return {
            "q": _w((lambda *s: torch.randn(*s, dtype=torch.bfloat16) * 0.05)(B, S, D)),
            "k": _w((lambda *s: torch.randn(*s, dtype=torch.bfloat16) * 0.4)(B, S, D)),
            "v": _w((lambda *s: torch.randn(*s, dtype=torch.bfloat16) * 0.4)(B, S, D)),
            "shift_k": _w((lambda *s: torch.randn(*s, dtype=torch.bfloat16) * 0.4)(D, ks) * 0.5),
            "ssm_k": _w((lambda *s: torch.randn(*s, dtype=torch.bfloat16) * 1.6)(D, S) * (1.0 / S ** 0.5)),
            "ssm_D": _w((lambda *s: torch.randn(*s, dtype=torch.bfloat16) * 0.4)(D)),
        }
    _B_in = _B_setup()

    return {
        "A_x": _A_in["x"],
        "A_wqkv": _A_in["wqkv"],
        "A_wo": _A_in["wo"],
        "B_q": _B_in["q"],
        "B_k": _B_in["k"],
        "B_v": _B_in["v"],
        "B_shift_k": _B_in["shift_k"],
        "B_ssm_k": _B_in["ssm_k"],
        "B_ssm_D": _B_in["ssm_D"],
        "gate_w": _w((lambda *s: torch.randn(*s, dtype=torch.bfloat16) * 0.05)(576) * 16.0),
    }

def reference(A_x, A_wqkv, A_wo, B_q, B_k, B_v, B_shift_k, B_ssm_k, B_ssm_D, gate_w):
    def _A_ref(x, wqkv, wo):
        B, S, D = x.shape
        H = 32
        Dh = D // H
        qkv = (lambda x, *d: x.permute(*d))((x @ wqkv).reshape(B, S, 3, H, Dh), 2, 0, 3, 1, 4)
        q, k, v = qkv[0], qkv[1], qkv[2]
        scores = torch.einsum("bhqd,bhkd->bhqk", q, k) * (Dh ** -0.5)
        p = _softmax(scores, axis=-1)
        o = torch.einsum("bhqk,bhkd->bhqd", p, v)
        return (lambda x, *d: x.permute(*d))(o, 0, 2, 1, 3).reshape(B, S, D) @ wo

    def _B_ref(q, k, v, shift_k, ssm_k, ssm_D):
        B, S, D = q.shape
        ks = shift_k.shape[-1]
        k_pad = _pad(k, ((0, 0), (ks - 1, 0), (0, 0)))
        k_shift = torch.zeros_like(k)
        for t in range(ks):
            k_shift = k_shift + k_pad[:, t:t + S, :] * shift_k[:, ks - 1 - t][None, None, :]
        kv = k_shift * v
        L = 2 * S
        kv_pad = _pad(kv, ((0, 0), (0, S), (0, 0)))
        kf_pad = _pad(ssm_k, ((0, 0), (0, S)))
        kv_f = (lambda x, axes=(-2,-1): torch.fft.rfft2(x.float(), dim=axes))(kv_pad[:, None, :, :], axes=(1, 2))
        kf_f = (lambda x, axes=(-2,-1): torch.fft.rfft2(x.float(), dim=axes))(kf_pad[:, None, :], axes=(1, 2))
        kf_f = (lambda x, *d: x.permute(*d))(kf_f, 1, 2, 0)
        y_f = kv_f * kf_f[None, :, :, :]
        y = (lambda x, s, axes=(-2,-1): torch.fft.irfft2(x, s=s, dim=axes).to(torch.bfloat16))(y_f, (1, L), axes=(1, 2))[:, 0, :S, :]
        y = y + kv * ssm_D[None, None, :]
        return q * y

    x = A_x
    g = _sigmoid((x * gate_w).sum(-1, keepdims=True))
    a = _A_ref(x, A_wqkv, A_wo)
    b = _B_ref(x, B_k, B_v, B_shift_k, B_ssm_k, B_ssm_D)
    return g * a + (1 - g) * b
```

```cpp
#include <hip/hip_runtime.h>
#include <stdint.h>

typedef unsigned short u16;
typedef unsigned int u32;
typedef float f32x4 __attribute__((ext_vector_type(4)));
typedef float f32x16 __attribute__((ext_vector_type(16)));
typedef u32 u32x2 __attribute__((ext_vector_type(2)));
typedef u32 u32x4 __attribute__((ext_vector_type(4)));
typedef u32 u32x8 __attribute__((ext_vector_type(8)));
typedef short s16x4 __attribute__((ext_vector_type(4)));
typedef __bf16 bf16x4 __attribute__((ext_vector_type(4)));
typedef __bf16 bf16x8 __attribute__((ext_vector_type(8)));
typedef __bf16 bf16x16 __attribute__((ext_vector_type(16)));
typedef __attribute__((address_space(3))) char lds_char;

constexpr int SEQ = 2048, DM = 576, NH = 32;
constexpr int CH = 9472;
constexpr int KVIMG = 16 * CH;
constexpr int UROW = 4112;
constexpr size_t WS_QM = 0;
constexpr size_t WS_QL = WS_QM + 2097152;
constexpr size_t WS_KV = WS_QL + 262144;
constexpr size_t WS_OM = WS_KV + (size_t)NH * KVIMG;
constexpr size_t WS_OL = WS_OM + 2097152;
constexpr size_t WS_UT = WS_OL + 262144;
constexpr size_t WS_YT = WS_UT + (size_t)DM * UROW;
constexpr size_t WS_G = WS_YT + (size_t)DM * 4096;
constexpr float QSCALE = 0.23570226039551584f * 1.4426950408889634f;
constexpr u32 OOR = 0x40000u;

__device__ __forceinline__ float bflo(u32 v) { return __uint_as_float(v << 16); }
__device__ __forceinline__ float bfhi(u32 v) { return __uint_as_float(v & 0xffff0000u); }
__device__ __forceinline__ u32 pk2(float lo, float hi) {
    typedef float f2 __attribute__((ext_vector_type(2)));
    typedef __bf16 b2 __attribute__((ext_vector_type(2)));
    f2 f = {lo, hi};
    return __builtin_bit_cast(u32, __builtin_convertvector(f, b2));
}
__device__ __forceinline__ u32 ldsaddr(const void* p) { return (u32)(uintptr_t)p; }
__device__ __forceinline__ u32 uni(u32 v) { return __builtin_amdgcn_readfirstlane(v); }
template <typename T> __device__ __forceinline__ T lds_ld(u32 a) { return *(const __attribute__((address_space(3))) T*)(uintptr_t)a; }
template <typename T> __device__ __forceinline__ void lds_st(u32 a, T v) { *(__attribute__((address_space(3))) T*)(uintptr_t)a = v; }
__device__ __forceinline__ s16x4 lds_tr(u32 a) {
    typedef short v4i16_t __attribute__((ext_vector_type(4)));
    return __builtin_amdgcn_ds_read_tr16_b64_v4i16((__attribute__((address_space(3))) v4i16_t*)(uintptr_t)a);
}
__device__ __forceinline__ bf16x8 cat8(s16x4 a, s16x4 b) {
    typedef short s8 __attribute__((ext_vector_type(8)));
    s8 r = {a[0], a[1], a[2], a[3], b[0], b[1], b[2], b[3]};
    return __builtin_bit_cast(bf16x8, r);
}
__device__ __forceinline__ void dma16(const void* gbase, u32 voff, u32 ldst) {
    u32 keep;
    asm volatile("s_mov_b32 %0, m0\n\ts_mov_b32 m0, %3\n\ts_nop 0\n\tglobal_load_lds_dwordx4 %1, %2\n\ts_mov_b32 m0, %0"
                 : "=&s"(keep) : "v"(voff), "s"(gbase), "s"(ldst) : "memory");
}
#define WAITV(n) asm volatile("s_waitcnt vmcnt(" #n ")" ::: "memory")
#define BAR() asm volatile("s_waitcnt lgkmcnt(0)\n\ts_barrier" ::: "memory")

__device__ __forceinline__ int vpos_main(int s) { return s < 4 ? 10 + s : (s < 12 ? s - 4 : s + 2); }
constexpr int VPOS_L0 = 8, VPOS_L1 = 18, VPOS_ONE = 9;

struct Ptrs {
    const u16* x; const u16* wqkv; const u16* wo; const u16* bk; const u16* bv;
    const u16* shk; const u16* ssmk; const u16* ssmd; const u16* gw;
    u16* out; char* ws;
};

__device__ __forceinline__ void phase_prep(const Ptrs& p, int b, u32 lds) {
    const int tid = threadIdx.x, lane = tid & 63, w = tid >> 6;
    const int tblk = b >> 3, cg = b & 7, t0 = tblk * 64, c0 = cg * 72;
    if (tid < 288) {
        const int tp = tid / 9, pc = tid - tp * 9, ch = c0 + pc * 8, t = t0 + 2 * tp;
        u32x4 kr[5], vr[2], wr[4];
#pragma unroll
        for (int i = 0; i < 5; ++i) {
            const int tt = t - 3 + i;
            u32x4 z = {0, 0, 0, 0};
            kr[i] = tt >= 0 ? *(const u32x4*)(p.bk + (size_t)tt * DM + ch) : z;
        }
#pragma unroll
        for (int i = 0; i < 2; ++i) vr[i] = *(const u32x4*)(p.bv + (size_t)(t + i) * DM + ch);
#pragma unroll
        for (int i = 0; i < 4; ++i) wr[i] = *(const u32x4*)(p.shk + (size_t)ch * 4 + i * 8);
#pragma unroll
        for (int c = 0; c < 8; ++c) {
            const u32 w01 = wr[c >> 1][(c & 1) * 2], w23 = wr[c >> 1][(c & 1) * 2 + 1];
            const float w0 = bflo(w01), w1 = bfhi(w01), w2 = bflo(w23), w3 = bfhi(w23);
            float k[5], v[2];
#pragma unroll
            for (int i = 0; i < 5; ++i) k[i] = (c & 1) ? bfhi(kr[i][c >> 1]) : bflo(kr[i][c >> 1]);
#pragma unroll
            for (int i = 0; i < 2; ++i) v[i] = (c & 1) ? bfhi(vr[i][c >> 1]) : bflo(vr[i][c >> 1]);
            const float u0 = (w3 * k[0] + w2 * k[1] + w1 * k[2] + w0 * k[3]) * v[0];
            const float u1 = (w3 * k[1] + w2 * k[2] + w1 * k[3] + w0 * k[4]) * v[1];
            lds_st<u32>(lds + (pc * 8 + c) * 132 + (31 - tp) * 4, pk2(u1, u0));
        }
    }
    {
        const int t = b * 8 + w;
        const u32x4 xa = *(const u32x4*)(p.x + (size_t)t * DM + lane * 8), ga = *(const u32x4*)(p.gw + lane * 8);
        u32x4 xb = {0, 0, 0, 0}, gb = {0, 0, 0, 0};
        if (lane < 8) { xb = *(const u32x4*)(p.x + (size_t)t * DM + 512 + lane * 8); gb = *(const u32x4*)(p.gw + 512 + lane * 8); }
        float s = 0.f;
#pragma unroll
        for (int i = 0; i < 4; ++i) s += bflo(xa[i]) * bflo(ga[i]) + bfhi(xa[i]) * bfhi(ga[i]) + bflo(xb[i]) * bflo(gb[i]) + bfhi(xb[i]) * bfhi(gb[i]);
#pragma unroll
        for (int o = 32; o > 0; o >>= 1) s += __shfl_xor(s, o);
        if (lane == 0) ((float*)(p.ws + WS_G))[t] = 1.f / (1.f + __expf(-s));
    }
    BAR();
    for (int i = tid; i < 576; i += 512) {
        const int c = i >> 3, pc = i & 7;
        u32x4 v;
#pragma unroll
        for (int j = 0; j < 4; ++j) v[j] = lds_ld<u32>(lds + c * 132 + pc * 16 + j * 4);
        *(u32x4*)(p.ws + WS_UT + (size_t)(c0 + c) * UROW + (size_t)(1984 - t0) * 2 + pc * 16) = v;
    }
    if (tblk == 0 && tid < 72) { const u32x4 z = {0, 0, 0, 0}; *(u32x4*)(p.ws + WS_UT + (size_t)(c0 + tid) * UROW + 4096) = z; }
    BAR();
}

__device__ __forceinline__ u32 w_chunk_addr(int a, int cj, int odd) {
    const int wb = 8 * cj + 8 * odd, pc = wb >> 4, half = (wb >> 3) & 1;
    const int spill = odd ? 0 : 4;
    if (pc == spill) return 3 * 4096 + a * 16 + half * 8;
    int idx;
    if (pc == 2) idx = a;
    else { const int j = odd ? (pc == 1 ? 0 : (pc == 3 ? 1 : 2)) : (pc == 3 ? 2 : pc); idx = j >= a ? j + 1 : j; }
    return a * 4096 + idx * 16 + half * 8;
}

__device__ __forceinline__ void phase_qkv(const Ptrs& p, int b, u32 lds) {
    const int tid = threadIdx.x, lane = tid & 63;
    const int w = uni(tid >> 6);
    const int xcd = b & 7, ii = b >> 3;
    const int tb = (xcd >> 1) * 4 + (ii & 3), hp = (xcd & 1) * 8 + (ii >> 2), odd = hp & 1;
    u32 xo[2], wo_[2];
#pragma unroll
    for (int i = 0; i < 2; ++i) {
        const int I = 2 * w + i;
        const int row = I * 8 + (lane >> 3), cs = (lane & 7) ^ ((row >> 1) & 7);
        xo[i] = row * 1152 + cs * 16;
        const int a = I >> 2, kr = 16 * (I & 3) + (lane >> 2), idx = lane & 3;
        int mat, pc;
        if (a < 3) { mat = a; if (idx == a) pc = 2; else { const int j = idx > a ? idx - 1 : idx; pc = odd ? (j == 0 ? 1 : (j == 1 ? 3 : 4)) : (j == 2 ? 3 : j); } }
        else { mat = idx < 3 ? idx : 0; pc = idx < 3 ? (odd ? 0 : 4) : 2; }
        wo_[i] = kr * 3456 + mat * 1152 + 72 * hp - 8 * odd + 16 * pc;
    }
    const char* xg = (const char*)p.x + (size_t)tb * 128 * 1152;
    const char* wg = (const char*)p.wqkv;
    auto issue = [&](int c) {
        const u32 slot = lds + (c & 3) * 32768;
        dma16(xg + c * 128, xo[0], slot + w * 2048);
        dma16(xg + c * 128, xo[1], slot + w * 2048 + 1024);
        dma16(wg + (size_t)c * 64 * 3456, wo_[0], slot + 16384 + w * 2048);
        dma16(wg + (size_t)c * 64 * 3456, wo_[1], slot + 16384 + w * 2048 + 1024);
    };
    issue(0); issue(1); issue(2);
    const int ft = w & 3, th = w >> 2;
    const int r = lane & 31, hh = lane >> 5;
    u32 xa[4];
#pragma unroll
    for (int s = 0; s < 4; ++s) xa[s] = (64 * th + r) * 128 + (((2 * s + hh) ^ ((r >> 1) & 7)) * 16);
    u32 wa;
    {
        const int G = lane >> 4, i = lane & 15, fh = G & 1, c = i & 3;
        const u32 rowo = (8 * (G >> 1) + (i >> 2)) * 64;
        if (ft == 2) wa = (fh == 0 && c < 3) ? 16384 + w_chunk_addr(c, 4, odd) + rowo : OOR;
        else {
            const int mc = ft == 3 ? c : (c == 1 ? 2 : (c == 2 ? 1 : c));
            wa = 16384 + w_chunk_addr(ft == 3 ? 2 : ft, 5 * fh + mc, odd) + rowo;
        }
    }
    f32x16 acc[2];
#pragma unroll
    for (int i = 0; i < 16; ++i) { acc[0][i] = 0.f; acc[1][i] = 0.f; }
#pragma unroll 1
    for (int c = 0; c < 9; ++c) {
        if (c < 7) WAITV(8); else if (c == 7) WAITV(4); else WAITV(0);
        BAR();
        if (c + 3 < 9) issue(c + 3);
        const u32 slot = lds + (c & 3) * 32768;
#pragma unroll
        for (int s = 0; s < 4; ++s) {
            const bf16x8 wf = cat8(lds_tr(slot + wa + s * 1024), lds_tr(slot + wa + s * 1024 + 256));
            const bf16x8 x0 = lds_ld<bf16x8>(slot + xa[s]), x1 = lds_ld<bf16x8>(slot + xa[s] + 4096);
            if (ft == 3) {
                acc[0] = __builtin_amdgcn_mfma_f32_32x32x16_bf16(x0, wf, acc[0], 0, 0, 0);
                acc[1] = __builtin_amdgcn_mfma_f32_32x32x16_bf16(x1, wf, acc[1], 0, 0, 0);
            } else {
                acc[0] = __builtin_amdgcn_mfma_f32_32x32x16_bf16(wf, x0, acc[0], 0, 0, 0);
                acc[1] = __builtin_amdgcn_mfma_f32_32x32x16_bf16(wf, x1, acc[1], 0, 0, 0);
            }
        }
    }
    BAR();
    const u32 stg = lds;
    const int h0 = 2 * hp;
    if (tid < 32) {
        const int fh = tid >> 4, j = tid & 15;
        const u32x4 one = {0x3f803f80u, 0x3f803f80u, 0x3f803f80u, 0x3f803f80u};
        lds_st<u32x4>(stg + fh * 5376 + 512 + (j >> 1) * 608 + (j & 1) * 304 + VPOS_ONE * 16, one);
    }
#pragma unroll
    for (int i = 0; i < 2; ++i) {
        const int tt = 2 * th + i;
        const f32x16 a = acc[i];
        if (ft == 0) {
#pragma unroll
            for (int fh = 0; fh < 2; ++fh) {
                u32x4 v;
#pragma unroll
                for (int j = 0; j < 4; ++j) v[j] = pk2(a[8 * fh + 2 * j] * QSCALE, a[8 * fh + 2 * j + 1] * QSCALE);
                *(u32x4*)(p.ws + WS_QM + ((size_t)(h0 + fh) * 64 + tb * 4 + tt) * 1024 + lane * 16) = v;
            }
        } else if (ft == 1) {
#pragma unroll
            for (int fh = 0; fh < 2; ++fh) {
                u32x4 v;
#pragma unroll
                for (int j = 0; j < 4; ++j) v[j] = pk2(a[8 * fh + 2 * j], a[8 * fh + 2 * j + 1]);
                *(u32x4*)(p.ws + WS_KV + (size_t)(h0 + fh) * KVIMG + tb * CH + tt * 1024 + lane * 16) = v;
            }
        } else if (ft == 2) {
            const int tok = 32 * tt + r;
#pragma unroll
            for (int fh = 0; fh < 2; ++fh) {
                if (hh == 0) {
                    *(u32*)(p.ws + WS_QL + ((size_t)(h0 + fh) * SEQ + tb * 128 + tok) * 4) = pk2(a[2 * fh] * QSCALE, a[2 * fh + 1] * QSCALE);
                    const u32 vv = pk2(a[4 + 2 * fh], a[5 + 2 * fh]);
                    const u32 vb = stg + fh * 5376 + 512 + (tok >> 4) * 608 + ((tok >> 3) & 1) * 304 + (tok & 7) * 2;
                    lds_st<u16>(vb + VPOS_L0 * 16, (u16)vv);
                    lds_st<u16>(vb + VPOS_L1 * 16, (u16)(vv >> 16));
                } else {
                    lds_st<u32>(stg + fh * 5376 + (tt >> 1) * 256 + r * 8 + (tt & 1) * 4, pk2(a[2 * fh], a[2 * fh + 1]));
                }
            }
        } else {
            const int fh = r >> 4, sg = r & 15;
#pragma unroll
            for (int g4 = 0; g4 < 4; ++g4) {
                u32x2 v = {pk2(a[4 * g4], a[4 * g4 + 1]), pk2(a[4 * g4 + 2], a[4 * g4 + 3])};
                lds_st<u32x2>(stg + fh * 5376 + 512 + (2 * tt + (g4 >> 1)) * 608 + (g4 & 1) * 304 + vpos_main(sg) * 16 + hh * 8, v);
            }
        }
    }
    BAR();
    for (int i = tid; i < 672; i += 512) {
        const int fh = i >= 336, j = i - fh * 336;
        *(u32x4*)(p.ws + WS_KV + (size_t)(h0 + fh) * KVIMG + tb * CH + 4096 + j * 16) = lds_ld<u32x4>(stg + fh * 5376 + j * 16);
    }
}

constexpr int HC = 4160, UC = 4544;
constexpr int CONV_CH = 4 * HC + 2 * UC + 32;
static_assert((4 * HC) % 256 == 0 && (UC + 32) % 256 == 224, "layout");

template <int NRHO>
__device__ __forceinline__ void conv_unit(u32 base, int rho0, float dval, int lane, f32x4 (&acc)[8]) {
    const int r = lane & 15, g = lane >> 4;
    const u32 aa = base + (r & 3) * HC + ((r >> 2) + g) * 16;
    const u32 ba = base + 4 * HC + ((r & 1) ? 0 : UC + 32) + (252 - 4 * (r >> 1) + g) * 16;
#pragma unroll 1
    for (int q = 0; q < NRHO; ++q) {
        const int rho = rho0 + q;
        bf16x8 A[8], B[8];
#pragma unroll
        for (int m = 0; m < 8; ++m) A[m] = lds_ld<bf16x8>(aa + rho * 64 + m * 512);
#pragma unroll
        for (int v = 0; v < 8; ++v) B[v] = lds_ld<bf16x8>(ba + rho * 64 - v * 512);
#pragma unroll
        for (int m = 0; m < 8; ++m)
#pragma unroll
            for (int n = m; n < 8; ++n) acc[n] = __builtin_amdgcn_mfma_f32_16x16x32_bf16(A[m], B[n - m], acc[n], 0, 0, 0);
        if (rho == 0) {
            typedef short s8 __attribute__((ext_vector_type(8)));
            const short db = (short)(__float_as_uint(dval) >> 16);
            s8 d;
#pragma unroll
            for (int j = 0; j < 8; ++j) d[j] = (8 * g + j == 30 - 2 * r) ? db : (short)0;
            const bf16x8 AD = __builtin_bit_cast(bf16x8, d);
#pragma unroll
            for (int n = 0; n < 8; ++n) acc[n] = __builtin_amdgcn_mfma_f32_16x16x32_bf16(AD, B[n], acc[n], 0, 0, 0);
        }
    }
}

__device__ __forceinline__ void conv_store(const Ptrs& p, int ch, int n, int lane, f32x4 a) {
    const int c = lane & 15, g = lane >> 4;
    f32x4 o;
#pragma unroll
    for (int j = 0; j < 4; ++j) o[j] = __shfl_xor(a[j], 1);
    u32x2 v;
    if (c & 1) { v[0] = pk2(o[2], a[2]); v[1] = pk2(o[3], a[3]); }
    else { v[0] = pk2(a[0], o[0]); v[1] = pk2(a[1], o[1]); }
    *(u32x2*)(p.ws + WS_YT + (size_t)ch * 4096 + (256 * n + 32 * (c >> 1) + 8 * g + 4 * (c & 1)) * 2) = v;
}

__device__ __forceinline__ void phase_conv(const Ptrs& p, int b, u32 lds) {
    const int tid = threadIdx.x, lane = tid & 63;
    const int w = uni(tid >> 6);
    const int nch = b < 64 ? 3 : 2;
    const int ch0 = b < 64 ? 3 * b : 192 + 2 * (b - 64);
    for (int I = w; I < nch * 26; I += 8) {
        const int cc = I / 26, j = I - cc * 26;
        const u32 base = lds + cc * CONV_CH;
        if (j < 16) {
            const int k = j >> 2, i = j & 3;
            int J = 4 + 64 * i + lane;
            const u32 dst = base + k * HC + (4 + 64 * i) * 16;
            if (i < 3 || lane < 63)
                dma16((const char*)p.ssmk + (size_t)(ch0 + cc) * 4096, 16 * J + 4 * k - 60, dst);
        } else {
            const int e = (j - 16) / 5, i = (j - 16) - 5 * e;
            const int J = 64 * i + lane;
            const u32 dst = base + 4 * HC + (e ? 0 : UC + 32) + 64 * i * 16;
            if (J < (e ? 257 : 256))
                dma16(p.ws + WS_UT + (size_t)(ch0 + cc) * UROW, 16 * J + (e ? 0 : 2), dst);
        }
    }
    for (int i = tid; i < nch * 16; i += 512) {
        const int cc = i >> 4, k = (i >> 2) & 3, J = i & 3;
        u32x4 v;
#pragma unroll
        for (int e = 0; e < 4; ++e) {
            const int i0 = 8 * J + 2 * k - 30 + 2 * e;
            const u32 lo = i0 >= 0 ? p.ssmk[(size_t)(ch0 + cc) * SEQ + i0] : 0, hi = i0 + 1 >= 0 ? p.ssmk[(size_t)(ch0 + cc) * SEQ + i0 + 1] : 0;
            v[e] = lo | (hi << 16);
        }
        lds_st<u32x4>(lds + cc * CONV_CH + k * HC + J * 16, v);
    }
    for (int i = tid; i < nch * 2 * 28; i += 512) {
        const int cc = i / 56, j = i - cc * 56, e = j / 28, J = 256 + (j - e * 28);
        const u32x4 z = {0, 0, 0, 0};
        if (e == 0 || J > 256) lds_st<u32x4>(lds + cc * CONV_CH + 4 * HC + (e ? 0 : UC + 32) + J * 16, z);
    }
    WAITV(0);
    BAR();
    f32x4 acc[8];
#pragma unroll
    for (int n = 0; n < 8; ++n) acc[n] = f32x4{0.f, 0.f, 0.f, 0.f};
    const bool active = w < 4 || nch == 3;
    const int cc = w < 4 ? (w >> 1) : 2;
    const int ch = ch0 + cc;
    if (active) {
        const float dval = bflo(p.ssmd[ch]);
        if (w < 4) conv_unit<4>(lds + cc * CONV_CH, 4 * (w & 1), dval, lane, acc);
        else conv_unit<2>(lds + cc * CONV_CH, 2 * (w & 3), dval, lane, acc);
    }
    BAR();
    const u32 red = lds;
    if (active) {
        if (w < 4) {
            const int hf = w & 1;
#pragma unroll
            for (int n = 0; n < 8; ++n) if ((n >> 2) != hf) lds_st<f32x4>(red + (w * 8 + n) * 1024 + lane * 16, acc[n]);
        } else {
#pragma unroll
            for (int n = 0; n < 8; ++n) if ((n >> 1) != (w & 3)) lds_st<f32x4>(red + (w * 8 + n) * 1024 + lane * 16, acc[n]);
        }
    }
    BAR();
    if (active) {
        if (w < 4) {
            const int hf = w & 1;
#pragma unroll
            for (int n = 0; n < 8; ++n)
                if ((n >> 2) == hf) conv_store(p, ch, n, lane, acc[n] + lds_ld<f32x4>(red + ((w ^ 1) * 8 + n) * 1024 + lane * 16));
        } else {
#pragma unroll
            for (int n = 0; n < 8; ++n)
                if ((n >> 1) == (w & 3)) {
                    f32x4 s = acc[n];
#pragma unroll
                    for (int o = 1; o < 4; ++o) s += lds_ld<f32x4>(red + ((4 + ((w + o) & 3)) * 8 + n) * 1024 + lane * 16);
                    conv_store(p, ch, n, lane, s);
                }
        }
    }
    BAR();
}

__device__ __forceinline__ f32x16 qk_tile(u32 aK, u32 aL, int t, bf16x8 qm, bf16x4 ql0, bf16x4 ql1, f32x16 c) {
    const u32 cb = (t >> 2) * CH;
    const bf16x4 kl = lds_ld<bf16x4>(aL + cb + 4096 + ((t & 3) >> 1) * 256);
    const bf16x8 km = lds_ld<bf16x8>(aK + cb + (t & 3) * 1024);
    typedef short s4 __attribute__((ext_vector_type(4)));
    c = __builtin_amdgcn_mfma_f32_32x32x8bf16_1k(__builtin_bit_cast(s4, kl), __builtin_bit_cast(s4, (t & 1) ? ql1 : ql0), c, 0, 0, 0);
    return __builtin_amdgcn_mfma_f32_32x32x16_bf16(km, qm, c, 0, 0, 0);
}

__device__ __forceinline__ f32x16 attn_true_max(u32 aK, u32 aL, u32 aV, u32x4 qmr, u32 qlr) {
    const bf16x8 qm = __builtin_bit_cast(bf16x8, qmr);
    const u32x2 q0 = {qlr, 0}, q1 = {0, qlr};
    const bf16x4 ql0 = __builtin_bit_cast(bf16x4, q0), ql1 = __builtin_bit_cast(bf16x4, q1);
    f32x16 z;
#pragma unroll
    for (int i = 0; i < 16; ++i) z[i] = 0.f;
    float m = -__builtin_inff();
#pragma unroll 1
    for (int t = 0; t < 64; ++t) {
        const f32x16 s = qk_tile(aK, aL, t, qm, ql0, ql1, z);
#pragma unroll
        for (int i = 0; i < 16; ++i) m = fmaxf(m, s[i]);
    }
    m = fmaxf(m, __shfl_xor(m, 32));
    f32x16 o = z;
#pragma unroll 1
    for (int t = 0; t < 64; ++t) {
        const f32x16 s = qk_tile(aK, aL, t, qm, ql0, ql1, z);
        const u32 vb = aV + (t >> 2) * CH + 4608 + (t & 3) * 1216;
#pragma unroll
        for (int hf = 0; hf < 2; ++hf) {
            u32x8 pr;
#pragma unroll
            for (int i = 0; i < 8; ++i) pr[i] = __float_as_uint(__builtin_amdgcn_exp2f(s[8 * hf + i] - m));
            o = __builtin_amdgcn_smfmac_f32_32x32x32_bf16(lds_ld<bf16x8>(vb + hf * 608), __builtin_bit_cast(bf16x16, pr), o, (int)0xDDDDDDDD, 0, 0);
        }
    }
    return o;
}

__device__ __forceinline__ void phase_attn(const Ptrs& p, int b, u32 lds) {
    const int tid = threadIdx.x, lane = tid & 63;
    const int w = uni(tid >> 6);
    const int xcd = b & 7, ii = b >> 3;
    const int h = xcd * 4 + (ii >> 3), qb = ii & 7, qt = qb * 8 + w;
    const int rho = lane & 31, ha = lane >> 5;
    int pos = -1;
    if (rho < 4) pos = 10 + rho; else if (rho < 8) pos = rho; else if (rho < 12) pos = rho - 8; else if (rho < 16) pos = rho + 2;
    else if (rho == 16) pos = VPOS_L0; else if (rho == 20) pos = VPOS_L1; else if (rho == 17 || rho == 21) pos = VPOS_ONE;
    const u32 aK = lds + lane * 16, aL = ha ? OOR : lds + rho * 8, aV = pos >= 0 ? lds + ha * 304 + pos * 16 : OOR;
    const char* qmp = p.ws + WS_QM + ((size_t)h * 64 + qt) * 1024;
    const char* qlp = p.ws + WS_QL + ((size_t)h * SEQ + qt * 32) * 4;
    const char* kvp = p.ws + WS_KV + (size_t)h * KVIMG + w * 5120;
    const u32 voff = lane * 16, vql = rho * 4, hmask = ha ? 0u : 0xffffffffu;
    const u32 m0base = lds + w * 5120;
    f32x16 o;
    u32x4 qm; u32 ql;
    asm volatile(
        "s_mov_b32 s21, m0\n\t"
        "global_load_dwordx4 v[172:175], %[voff], %[qmp]\n\t"
        "global_load_dword v176, %[vql], %[qlp]\n\t"
        "s_mov_b64 s[22:23], %[kvp]\n\t"
        "s_add_u32 s22, s22, 2048\n\t"
        "s_addc_u32 s23, s23, 0\n\t"
        "s_add_u32 s24, %[m0b], 2048\n\t"
        "s_mov_b32 m0, s24\n\t"
        "v_mov_b32 v184, %[voff]\n\t"
        "global_load_lds_dwordx4 v184, s[22:23] offset:-2048\n\t"
        "global_load_lds_dwordx4 v184, s[22:23] offset:-1024\n\t"
        "global_load_lds_dwordx4 v184, s[22:23] offset:0\n\t"
        "global_load_lds_dwordx4 v184, s[22:23] offset:1024\n\t"
        "global_load_lds_dwordx4 v184, s[22:23] offset:2048\n\t"
        "s_add_u32 s22, s22, 37888\n\t"
        "s_addc_u32 s23, s23, 0\n\t"
        "s_add_u32 s24, s24, 37888\n\t"
        "v_mov_b32 v180, 0xdddddddd\n\t"
        "v_mov_b32 v128, 0\n\t"
        "v_mov_b32 v129, 0\n\t"
        "v_mov_b32 v130, 0\n\t"
        "v_mov_b32 v131, 0\n\t"
        "v_mov_b32 v132, 0\n\t"
        "v_mov_b32 v133, 0\n\t"
        "v_mov_b32 v134, 0\n\t"
        "v_mov_b32 v135, 0\n\t"
        "v_mov_b32 v136, 0\n\t"
        "v_mov_b32 v137, 0\n\t"
        "v_mov_b32 v138, 0\n\t"
        "v_mov_b32 v139, 0\n\t"
        "v_mov_b32 v140, 0\n\t"
        "v_mov_b32 v141, 0\n\t"
        "v_mov_b32 v142, 0\n\t"
        "v_mov_b32 v143, 0\n\t"
        "v_mov_b32 v120, 0\n\t"
        "v_mov_b32 v121, 0\n\t"
        "v_mov_b32 v122, 0\n\t"
        "v_mov_b32 v123, 0\n\t"
        "v_mov_b32 v124, 0\n\t"
        "v_mov_b32 v125, 0\n\t"
        "v_mov_b32 v126, 0\n\t"
        "v_mov_b32 v127, 0\n\t"
        "v_mov_b32 v164, 0\n\t"
        "v_mov_b32 v165, 0\n\t"
        "v_mov_b32 v166, 0\n\t"
        "v_mov_b32 v167, 0\n\t"
        "v_mov_b32 v181, %[aK]\n\t"
        "v_mov_b32 v182, %[aL]\n\t"
        "v_mov_b32 v183, %[aV]\n\t"
        "v_mov_b32 v177, 0\n\t"
        "v_mov_b32 v178, 0\n\t"
        "s_waitcnt vmcnt(5)\n\t"
        "v_and_b32 v176, v176, %[hm]\n\t"
        "v_mov_b32 v179, v176\n\t"
        "s_waitcnt vmcnt(0)\n\t"
        "s_barrier\n\t"
        "ds_read_b64 v[168:169], v182 offset:4096\n\t"
        "ds_read_b128 v[144:147], v181 offset:0\n\t"
        "ds_read_b64 v[170:171], v182 offset:4352\n\t"
        "ds_read_b128 v[148:151], v181 offset:1024\n\t"
        "ds_read_b128 v[152:155], v183 offset:4608\n\t"
        "s_waitcnt lgkmcnt(3)\n\t"
        "v_mfma_f32_32x32x8bf16_1k v[64:79], v[168:169], v[176:177], 0\n\t"
        "v_mfma_f32_32x32x16_bf16 v[64:79], v[144:147], v[172:175], v[64:79]\n\t"
        "ds_read_b128 v[160:163], v183 offset:5216\n\t"
        "ds_read_b128 v[144:147], v181 offset:2048\n\t"
        "ds_read_b128 v[156:159], v183 offset:5824\n\t"
        "s_nop 7\n\t"
        "s_mov_b32 s20, 0\n\t"
        "1:\n\t"
        "s_mov_b32 m0, s24\n\t"
        "s_waitcnt lgkmcnt(3)\n\t"
        "v_mfma_f32_32x32x8bf16_1k v[80:95], v[168:169], v[178:179], 0\n\t"
        "ds_read_b64 v[168:169], v182 offset:13568\n\t"
        "v_exp_f32 v64, v64\n\t"
        "v_exp_f32 v65, v65\n\t"
        "v_exp_f32 v66, v66\n\t"
        "v_exp_f32 v67, v67\n\t"
        "v_smfmac_f32_32x32x32_bf16 v[128:143], v[164:167], v[120:127], v180\n\t"
        "ds_read_b128 v[164:167], v183 offset:6432\n\t"
        "v_exp_f32 v68, v68\n\t"
        "v_exp_f32 v69, v69\n\t"
        "v_exp_f32 v70, v70\n\t"
        "v_exp_f32 v71, v71\n\t"
        "v_mfma_f32_32x32x16_bf16 v[80:95], v[148:151], v[172:175], v[80:95]\n\t"
        "ds_read_b128 v[148:151], v181 offset:3072\n\t"
        "v_exp_f32 v72, v72\n\t"
        "v_exp_f32 v73, v73\n\t"
        "v_exp_f32 v74, v74\n\t"
        "v_exp_f32 v75, v75\n\t"
        "v_smfmac_f32_32x32x32_bf16 v[128:143], v[152:155], v[64:71], v180\n\t"
        "ds_read_b128 v[152:155], v183 offset:7040\n\t"
        "v_exp_f32 v76, v76\n\t"
        "v_exp_f32 v77, v77\n\t"
        "v_exp_f32 v78, v78\n\t"
        "v_exp_f32 v79, v79\n\t"
        "s_waitcnt lgkmcnt(4)\n\t"
        "v_mfma_f32_32x32x8bf16_1k v[96:111], v[170:171], v[176:177], 0\n\t"
        "global_load_lds_dwordx4 v184, s[22:23] offset:-2048\n\t"
        "v_exp_f32 v80, v80\n\t"
        "v_exp_f32 v81, v81\n\t"
        "v_exp_f32 v82, v82\n\t"
        "v_exp_f32 v83, v83\n\t"
        "v_smfmac_f32_32x32x32_bf16 v[128:143], v[160:163], v[72:79], v180\n\t"
        "ds_read_b128 v[160:163], v183 offset:7648\n\t"
        "v_exp_f32 v84, v84\n\t"
        "v_exp_f32 v85, v85\n\t"
        "v_exp_f32 v86, v86\n\t"
        "v_exp_f32 v87, v87\n\t"
        "v_mfma_f32_32x32x16_bf16 v[96:111], v[144:147], v[172:175], v[96:111]\n\t"
        "ds_read_b128 v[144:147], v181 offset:9472\n\t"
        "v_exp_f32 v88, v88\n\t"
        "v_exp_f32 v89, v89\n\t"
        "v_exp_f32 v90, v90\n\t"
        "v_exp_f32 v91, v91\n\t"
        "v_smfmac_f32_32x32x32_bf16 v[128:143], v[156:159], v[80:87], v180\n\t"
        "ds_read_b128 v[156:159], v183 offset:8256\n\t"
        "v_exp_f32 v92, v92\n\t"
        "v_exp_f32 v93, v93\n\t"
        "v_exp_f32 v94, v94\n\t"
        "v_exp_f32 v95, v95\n\t"
        "s_waitcnt lgkmcnt(3)\n\t"
        "v_mfma_f32_32x32x8bf16_1k v[112:127], v[170:171], v[178:179], 0\n\t"
        "ds_read_b64 v[170:171], v182 offset:13824\n\t"
        "v_exp_f32 v96, v96\n\t"
        "v_exp_f32 v97, v97\n\t"
        "v_exp_f32 v98, v98\n\t"
        "v_exp_f32 v99, v99\n\t"
        "v_smfmac_f32_32x32x32_bf16 v[128:143], v[164:167], v[88:95], v180\n\t"
        "ds_read_b128 v[164:167], v183 offset:8864\n\t"
        "v_exp_f32 v100, v100\n\t"
        "v_exp_f32 v101, v101\n\t"
        "v_exp_f32 v102, v102\n\t"
        "v_exp_f32 v103, v103\n\t"
        "v_mfma_f32_32x32x16_bf16 v[112:127], v[148:151], v[172:175], v[112:127]\n\t"
        "ds_read_b128 v[148:151], v181 offset:10496\n\t"
        "v_exp_f32 v104, v104\n\t"
        "v_exp_f32 v105, v105\n\t"
        "v_exp_f32 v106, v106\n\t"
        "v_exp_f32 v107, v107\n\t"
        "v_smfmac_f32_32x32x32_bf16 v[128:143], v[152:155], v[96:103], v180\n\t"
        "ds_read_b128 v[152:155], v183 offset:14080\n\t"
        "v_exp_f32 v108, v108\n\t"
        "v_exp_f32 v109, v109\n\t"
        "v_exp_f32 v110, v110\n\t"
        "v_exp_f32 v111, v111\n\t"
        "s_waitcnt lgkmcnt(4)\n\t"
        "v_mfma_f32_32x32x8bf16_1k v[64:79], v[168:169], v[176:177], 0\n\t"
        "global_load_lds_dwordx4 v184, s[22:23] offset:-1024\n\t"
        "v_exp_f32 v112, v112\n\t"
        "v_exp_f32 v113, v113\n\t"
        "v_exp_f32 v114, v114\n\t"
        "v_exp_f32 v115, v115\n\t"
        "v_smfmac_f32_32x32x32_bf16 v[128:143], v[160:163], v[104:111], v180\n\t"
        "ds_read_b128 v[160:163], v183 offset:14688\n\t"
        "v_exp_f32 v116, v116\n\t"
        "v_exp_f32 v117, v117\n\t"
        "v_exp_f32 v118, v118\n\t"
        "v_exp_f32 v119, v119\n\t"
        "v_mfma_f32_32x32x16_bf16 v[64:79], v[144:147], v[172:175], v[64:79]\n\t"
        "ds_read_b128 v[144:147], v181 offset:11520\n\t"
        "v_exp_f32 v120, v120\n\t"
        "v_exp_f32 v121, v121\n\t"
        "v_exp_f32 v122, v122\n\t"
        "v_exp_f32 v123, v123\n\t"
        "v_smfmac_f32_32x32x32_bf16 v[128:143], v[156:159], v[112:119], v180\n\t"
        "ds_read_b128 v[156:159], v183 offset:15296\n\t"
        "v_exp_f32 v124, v124\n\t"
        "v_exp_f32 v125, v125\n\t"
        "v_exp_f32 v126, v126\n\t"
        "v_exp_f32 v127, v127\n\t"
        "s_waitcnt lgkmcnt(3)\n\t"
        "v_mfma_f32_32x32x8bf16_1k v[80:95], v[168:169], v[178:179], 0\n\t"
        "ds_read_b64 v[168:169], v182 offset:23040\n\t"
        "v_exp_f32 v64, v64\n\t"
        "v_exp_f32 v65, v65\n\t"
        "v_exp_f32 v66, v66\n\t"
        "v_exp_f32 v67, v67\n\t"
        "v_smfmac_f32_32x32x32_bf16 v[128:143], v[164:167], v[120:127], v180\n\t"
        "ds_read_b128 v[164:167], v183 offset:15904\n\t"
        "v_exp_f32 v68, v68\n\t"
        "v_exp_f32 v69, v69\n\t"
        "v_exp_f32 v70, v70\n\t"
        "v_exp_f32 v71, v71\n\t"
        "v_mfma_f32_32x32x16_bf16 v[80:95], v[148:151], v[172:175], v[80:95]\n\t"
        "ds_read_b128 v[148:151], v181 offset:12544\n\t"
        "v_exp_f32 v72, v72\n\t"
        "v_exp_f32 v73, v73\n\t"
        "v_exp_f32 v74, v74\n\t"
        "v_exp_f32 v75, v75\n\t"
        "v_smfmac_f32_32x32x32_bf16 v[128:143], v[152:155], v[64:71], v180\n\t"
        "ds_read_b128 v[152:155], v183 offset:16512\n\t"
        "v_exp_f32 v76, v76\n\t"
        "v_exp_f32 v77, v77\n\t"
        "v_exp_f32 v78, v78\n\t"
        "v_exp_f32 v79, v79\n\t"
        "s_waitcnt lgkmcnt(4)\n\t"
        "v_mfma_f32_32x32x8bf16_1k v[96:111], v[170:171], v[176:177], 0\n\t"
        "global_load_lds_dwordx4 v184, s[22:23] offset:0\n\t"
        "v_exp_f32 v80, v80\n\t"
        "v_exp_f32 v81, v81\n\t"
        "v_exp_f32 v82, v82\n\t"
        "v_exp_f32 v83, v83\n\t"
        "v_smfmac_f32_32x32x32_bf16 v[128:143], v[160:163], v[72:79], v180\n\t"
        "ds_read_b128 v[160:163], v183 offset:17120\n\t"
        "v_exp_f32 v84, v84\n\t"
        "v_exp_f32 v85, v85\n\t"
        "v_exp_f32 v86, v86\n\t"
        "v_exp_f32 v87, v87\n\t"
        "v_mfma_f32_32x32x16_bf16 v[96:111], v[144:147], v[172:175], v[96:111]\n\t"
        "ds_read_b128 v[144:147], v181 offset:18944\n\t"
        "v_exp_f32 v88, v88\n\t"
        "v_exp_f32 v89, v89\n\t"
        "v_exp_f32 v90, v90\n\t"
        "v_exp_f32 v91, v91\n\t"
        "v_smfmac_f32_32x32x32_bf16 v[128:143], v[156:159], v[80:87], v180\n\t"
        "ds_read_b128 v[156:159], v183 offset:17728\n\t"
        "v_exp_f32 v92, v92\n\t"
        "v_exp_f32 v93, v93\n\t"
        "v_exp_f32 v94, v94\n\t"
        "v_exp_f32 v95, v95\n\t"
        "s_waitcnt lgkmcnt(3)\n\t"
        "v_mfma_f32_32x32x8bf16_1k v[112:127], v[170:171], v[178:179], 0\n\t"
        "ds_read_b64 v[170:171], v182 offset:23296\n\t"
        "v_exp_f32 v96, v96\n\t"
        "v_exp_f32 v97, v97\n\t"
        "v_exp_f32 v98, v98\n\t"
        "v_exp_f32 v99, v99\n\t"
        "v_smfmac_f32_32x32x32_bf16 v[128:143], v[164:167], v[88:95], v180\n\t"
        "ds_read_b128 v[164:167], v183 offset:18336\n\t"
        "v_exp_f32 v100, v100\n\t"
        "v_exp_f32 v101, v101\n\t"
        "v_exp_f32 v102, v102\n\t"
        "v_exp_f32 v103, v103\n\t"
        "v_mfma_f32_32x32x16_bf16 v[112:127], v[148:151], v[172:175], v[112:127]\n\t"
        "ds_read_b128 v[148:151], v181 offset:19968\n\t"
        "v_exp_f32 v104, v104\n\t"
        "v_exp_f32 v105, v105\n\t"
        "v_exp_f32 v106, v106\n\t"
        "v_exp_f32 v107, v107\n\t"
        "v_smfmac_f32_32x32x32_bf16 v[128:143], v[152:155], v[96:103], v180\n\t"
        "ds_read_b128 v[152:155], v183 offset:23552\n\t"
        "v_exp_f32 v108, v108\n\t"
        "v_exp_f32 v109, v109\n\t"
        "v_exp_f32 v110, v110\n\t"
        "v_exp_f32 v111, v111\n\t"
        "s_waitcnt lgkmcnt(4)\n\t"
        "v_mfma_f32_32x32x8bf16_1k v[64:79], v[168:169], v[176:177], 0\n\t"
        "global_load_lds_dwordx4 v184, s[22:23] offset:1024\n\t"
        "v_exp_f32 v112, v112\n\t"
        "v_exp_f32 v113, v113\n\t"
        "v_exp_f32 v114, v114\n\t"
        "v_exp_f32 v115, v115\n\t"
        "v_smfmac_f32_32x32x32_bf16 v[128:143], v[160:163], v[104:111], v180\n\t"
        "ds_read_b128 v[160:163], v183 offset:24160\n\t"
        "v_exp_f32 v116, v116\n\t"
        "v_exp_f32 v117, v117\n\t"
        "v_exp_f32 v118, v118\n\t"
        "v_exp_f32 v119, v119\n\t"
        "v_mfma_f32_32x32x16_bf16 v[64:79], v[144:147], v[172:175], v[64:79]\n\t"
        "ds_read_b128 v[144:147], v181 offset:20992\n\t"
        "v_exp_f32 v120, v120\n\t"
        "v_exp_f32 v121, v121\n\t"
        "v_exp_f32 v122, v122\n\t"
        "v_exp_f32 v123, v123\n\t"
        "v_smfmac_f32_32x32x32_bf16 v[128:143], v[156:159], v[112:119], v180\n\t"
        "ds_read_b128 v[156:159], v183 offset:24768\n\t"
        "v_exp_f32 v124, v124\n\t"
        "v_exp_f32 v125, v125\n\t"
        "v_exp_f32 v126, v126\n\t"
        "v_exp_f32 v127, v127\n\t"
        "s_waitcnt lgkmcnt(3)\n\t"
        "v_mfma_f32_32x32x8bf16_1k v[80:95], v[168:169], v[178:179], 0\n\t"
        "ds_read_b64 v[168:169], v182 offset:32512\n\t"
        "v_exp_f32 v64, v64\n\t"
        "v_exp_f32 v65, v65\n\t"
        "v_exp_f32 v66, v66\n\t"
        "v_exp_f32 v67, v67\n\t"
        "v_smfmac_f32_32x32x32_bf16 v[128:143], v[164:167], v[120:127], v180\n\t"
        "ds_read_b128 v[164:167], v183 offset:25376\n\t"
        "v_exp_f32 v68, v68\n\t"
        "v_exp_f32 v69, v69\n\t"
        "v_exp_f32 v70, v70\n\t"
        "v_exp_f32 v71, v71\n\t"
        "v_mfma_f32_32x32x16_bf16 v[80:95], v[148:151], v[172:175], v[80:95]\n\t"
        "ds_read_b128 v[148:151], v181 offset:22016\n\t"
        "v_exp_f32 v72, v72\n\t"
        "v_exp_f32 v73, v73\n\t"
        "v_exp_f32 v74, v74\n\t"
        "v_exp_f32 v75, v75\n\t"
        "v_smfmac_f32_32x32x32_bf16 v[128:143], v[152:155], v[64:71], v180\n\t"
        "ds_read_b128 v[152:155], v183 offset:25984\n\t"
        "v_exp_f32 v76, v76\n\t"
        "v_exp_f32 v77, v77\n\t"
        "v_exp_f32 v78, v78\n\t"
        "v_exp_f32 v79, v79\n\t"
        "s_waitcnt lgkmcnt(4)\n\t"
        "v_mfma_f32_32x32x8bf16_1k v[96:111], v[170:171], v[176:177], 0\n\t"
        "global_load_lds_dwordx4 v184, s[22:23] offset:2048\n\t"
        "v_exp_f32 v80, v80\n\t"
        "v_exp_f32 v81, v81\n\t"
        "v_exp_f32 v82, v82\n\t"
        "v_exp_f32 v83, v83\n\t"
        "v_smfmac_f32_32x32x32_bf16 v[128:143], v[160:163], v[72:79], v180\n\t"
        "ds_read_b128 v[160:163], v183 offset:26592\n\t"
        "v_exp_f32 v84, v84\n\t"
        "v_exp_f32 v85, v85\n\t"
        "v_exp_f32 v86, v86\n\t"
        "v_exp_f32 v87, v87\n\t"
        "v_mfma_f32_32x32x16_bf16 v[96:111], v[144:147], v[172:175], v[96:111]\n\t"
        "ds_read_b128 v[144:147], v181 offset:28416\n\t"
        "v_exp_f32 v88, v88\n\t"
        "v_exp_f32 v89, v89\n\t"
        "v_exp_f32 v90, v90\n\t"
        "v_exp_f32 v91, v91\n\t"
        "v_smfmac_f32_32x32x32_bf16 v[128:143], v[156:159], v[80:87], v180\n\t"
        "ds_read_b128 v[156:159], v183 offset:27200\n\t"
        "v_exp_f32 v92, v92\n\t"
        "v_exp_f32 v93, v93\n\t"
        "v_exp_f32 v94, v94\n\t"
        "v_exp_f32 v95, v95\n\t"
        "s_waitcnt lgkmcnt(3)\n\t"
        "v_mfma_f32_32x32x8bf16_1k v[112:127], v[170:171], v[178:179], 0\n\t"
        "ds_read_b64 v[170:171], v182 offset:32768\n\t"
        "v_exp_f32 v96, v96\n\t"
        "v_exp_f32 v97, v97\n\t"
        "v_exp_f32 v98, v98\n\t"
        "v_exp_f32 v99, v99\n\t"
        "v_smfmac_f32_32x32x32_bf16 v[128:143], v[164:167], v[88:95], v180\n\t"
        "ds_read_b128 v[164:167], v183 offset:27808\n\t"
        "v_exp_f32 v100, v100\n\t"
        "v_exp_f32 v101, v101\n\t"
        "v_exp_f32 v102, v102\n\t"
        "v_exp_f32 v103, v103\n\t"
        "v_mfma_f32_32x32x16_bf16 v[112:127], v[148:151], v[172:175], v[112:127]\n\t"
        "ds_read_b128 v[148:151], v181 offset:29440\n\t"
        "v_exp_f32 v104, v104\n\t"
        "v_exp_f32 v105, v105\n\t"
        "v_exp_f32 v106, v106\n\t"
        "v_exp_f32 v107, v107\n\t"
        "v_smfmac_f32_32x32x32_bf16 v[128:143], v[152:155], v[96:103], v180\n\t"
        "ds_read_b128 v[152:155], v183 offset:33024\n\t"
        "v_exp_f32 v108, v108\n\t"
        "v_exp_f32 v109, v109\n\t"
        "v_exp_f32 v110, v110\n\t"
        "v_exp_f32 v111, v111\n\t"
        "s_waitcnt lgkmcnt(4)\n\t"
        "v_mfma_f32_32x32x8bf16_1k v[64:79], v[168:169], v[176:177], 0\n\t"
        "v_exp_f32 v112, v112\n\t"
        "v_exp_f32 v113, v113\n\t"
        "v_exp_f32 v114, v114\n\t"
        "v_exp_f32 v115, v115\n\t"
        "v_smfmac_f32_32x32x32_bf16 v[128:143], v[160:163], v[104:111], v180\n\t"
        "ds_read_b128 v[160:163], v183 offset:33632\n\t"
        "v_exp_f32 v116, v116\n\t"
        "v_exp_f32 v117, v117\n\t"
        "v_exp_f32 v118, v118\n\t"
        "v_exp_f32 v119, v119\n\t"
        "v_mfma_f32_32x32x16_bf16 v[64:79], v[144:147], v[172:175], v[64:79]\n\t"
        "ds_read_b128 v[144:147], v181 offset:30464\n\t"
        "v_exp_f32 v120, v120\n\t"
        "v_exp_f32 v121, v121\n\t"
        "v_exp_f32 v122, v122\n\t"
        "v_exp_f32 v123, v123\n\t"
        "v_smfmac_f32_32x32x32_bf16 v[128:143], v[156:159], v[112:119], v180\n\t"
        "ds_read_b128 v[156:159], v183 offset:34240\n\t"
        "v_exp_f32 v124, v124\n\t"
        "v_exp_f32 v125, v125\n\t"
        "v_exp_f32 v126, v126\n\t"
        "v_exp_f32 v127, v127\n\t"
        "s_waitcnt vmcnt(0)\n\t"
        "s_barrier\n\t"
        "s_waitcnt lgkmcnt(3)\n\t"
        "v_mfma_f32_32x32x8bf16_1k v[80:95], v[168:169], v[178:179], 0\n\t"
        "ds_read_b64 v[168:169], v182 offset:41984\n\t"
        "v_exp_f32 v64, v64\n\t"
        "v_exp_f32 v65, v65\n\t"
        "v_exp_f32 v66, v66\n\t"
        "v_exp_f32 v67, v67\n\t"
        "v_smfmac_f32_32x32x32_bf16 v[128:143], v[164:167], v[120:127], v180\n\t"
        "ds_read_b128 v[164:167], v183 offset:34848\n\t"
        "v_exp_f32 v68, v68\n\t"
        "v_exp_f32 v69, v69\n\t"
        "v_exp_f32 v70, v70\n\t"
        "v_exp_f32 v71, v71\n\t"
        "v_mfma_f32_32x32x16_bf16 v[80:95], v[148:151], v[172:175], v[80:95]\n\t"
        "ds_read_b128 v[148:151], v181 offset:31488\n\t"
        "v_exp_f32 v72, v72\n\t"
        "v_exp_f32 v73, v73\n\t"
        "v_exp_f32 v74, v74\n\t"
        "v_exp_f32 v75, v75\n\t"
        "v_smfmac_f32_32x32x32_bf16 v[128:143], v[152:155], v[64:71], v180\n\t"
        "ds_read_b128 v[152:155], v183 offset:35456\n\t"
        "v_exp_f32 v76, v76\n\t"
        "v_exp_f32 v77, v77\n\t"
        "v_exp_f32 v78, v78\n\t"
        "v_exp_f32 v79, v79\n\t"
        "s_waitcnt lgkmcnt(4)\n\t"
        "v_mfma_f32_32x32x8bf16_1k v[96:111], v[170:171], v[176:177], 0\n\t"
        "v_exp_f32 v80, v80\n\t"
        "v_exp_f32 v81, v81\n\t"
        "v_exp_f32 v82, v82\n\t"
        "v_exp_f32 v83, v83\n\t"
        "v_smfmac_f32_32x32x32_bf16 v[128:143], v[160:163], v[72:79], v180\n\t"
        "ds_read_b128 v[160:163], v183 offset:36064\n\t"
        "v_exp_f32 v84, v84\n\t"
        "v_exp_f32 v85, v85\n\t"
        "v_exp_f32 v86, v86\n\t"
        "v_exp_f32 v87, v87\n\t"
        "v_mfma_f32_32x32x16_bf16 v[96:111], v[144:147], v[172:175], v[96:111]\n\t"
        "ds_read_b128 v[144:147], v181 offset:37888\n\t"
        "v_exp_f32 v88, v88\n\t"
        "v_exp_f32 v89, v89\n\t"
        "v_exp_f32 v90, v90\n\t"
        "v_exp_f32 v91, v91\n\t"
        "v_smfmac_f32_32x32x32_bf16 v[128:143], v[156:159], v[80:87], v180\n\t"
        "ds_read_b128 v[156:159], v183 offset:36672\n\t"
        "v_exp_f32 v92, v92\n\t"
        "v_exp_f32 v93, v93\n\t"
        "v_exp_f32 v94, v94\n\t"
        "v_exp_f32 v95, v95\n\t"
        "s_waitcnt lgkmcnt(3)\n\t"
        "v_mfma_f32_32x32x8bf16_1k v[112:127], v[170:171], v[178:179], 0\n\t"
        "ds_read_b64 v[170:171], v182 offset:42240\n\t"
        "v_exp_f32 v96, v96\n\t"
        "v_exp_f32 v97, v97\n\t"
        "v_exp_f32 v98, v98\n\t"
        "v_exp_f32 v99, v99\n\t"
        "v_smfmac_f32_32x32x32_bf16 v[128:143], v[164:167], v[88:95], v180\n\t"
        "ds_read_b128 v[164:167], v183 offset:37280\n\t"
        "v_exp_f32 v100, v100\n\t"
        "v_exp_f32 v101, v101\n\t"
        "v_exp_f32 v102, v102\n\t"
        "v_exp_f32 v103, v103\n\t"
        "v_mfma_f32_32x32x16_bf16 v[112:127], v[148:151], v[172:175], v[112:127]\n\t"
        "ds_read_b128 v[148:151], v181 offset:38912\n\t"
        "v_exp_f32 v104, v104\n\t"
        "v_exp_f32 v105, v105\n\t"
        "v_exp_f32 v106, v106\n\t"
        "v_exp_f32 v107, v107\n\t"
        "v_smfmac_f32_32x32x32_bf16 v[128:143], v[152:155], v[96:103], v180\n\t"
        "ds_read_b128 v[152:155], v183 offset:42496\n\t"
        "v_exp_f32 v108, v108\n\t"
        "v_exp_f32 v109, v109\n\t"
        "v_exp_f32 v110, v110\n\t"
        "v_exp_f32 v111, v111\n\t"
        "s_waitcnt lgkmcnt(4)\n\t"
        "v_mfma_f32_32x32x8bf16_1k v[64:79], v[168:169], v[176:177], 0\n\t"
        "v_exp_f32 v112, v112\n\t"
        "v_exp_f32 v113, v113\n\t"
        "v_exp_f32 v114, v114\n\t"
        "v_exp_f32 v115, v115\n\t"
        "v_smfmac_f32_32x32x32_bf16 v[128:143], v[160:163], v[104:111], v180\n\t"
        "ds_read_b128 v[160:163], v183 offset:43104\n\t"
        "v_exp_f32 v116, v116\n\t"
        "v_exp_f32 v117, v117\n\t"
        "v_exp_f32 v118, v118\n\t"
        "v_exp_f32 v119, v119\n\t"
        "v_mfma_f32_32x32x16_bf16 v[64:79], v[144:147], v[172:175], v[64:79]\n\t"
        "ds_read_b128 v[144:147], v181 offset:39936\n\t"
        "v_exp_f32 v120, v120\n\t"
        "v_exp_f32 v121, v121\n\t"
        "v_exp_f32 v122, v122\n\t"
        "v_exp_f32 v123, v123\n\t"
        "v_smfmac_f32_32x32x32_bf16 v[128:143], v[156:159], v[112:119], v180\n\t"
        "ds_read_b128 v[156:159], v183 offset:43712\n\t"
        "v_exp_f32 v124, v124\n\t"
        "v_exp_f32 v125, v125\n\t"
        "v_exp_f32 v126, v126\n\t"
        "v_exp_f32 v127, v127\n\t"
        "v_add_u32 v181, 37888, v181\n\t"
        "v_add_u32 v182, 37888, v182\n\t"
        "v_add_u32 v183, 37888, v183\n\t"
        "s_cmp_lt_u32 s20, 2\n\t"
        "s_cselect_b32 s25, 37888, 0\n\t"
        "s_add_u32 s22, s22, s25\n\t"
        "s_addc_u32 s23, s23, 0\n\t"
        "s_add_u32 s24, s24, s25\n\t"
        "s_add_u32 s20, s20, 1\n\t"
        "s_cmp_lg_u32 s20, 4\n\t"
        "s_cbranch_scc1 1b\n\t"
        "s_waitcnt lgkmcnt(0)\n\t"
        "v_smfmac_f32_32x32x32_bf16 v[128:143], v[164:167], v[120:127], v180\n\t"
        "s_mov_b32 m0, s21\n\t"
        "s_nop 15\n\t"
        "s_nop 3\n\t"
        : "={v[128:143]}"(o), "={v[172:175]}"(qm), "={v176}"(ql)
        : [voff] "v"(voff), [vql] "v"(vql), [qmp] "s"(qmp), [qlp] "s"(qlp), [kvp] "s"(kvp), [m0b] "s"(m0base), [wv] "s"(w),
          [hm] "v"(hmask), [aK] "v"(aK), [aL] "v"(aL), [aV] "v"(aV)
        : "memory", "scc", "vcc", "s20", "s21", "s22", "s23", "s24", "s25", "s26", "s27",
          "v64", "v65", "v66", "v67", "v68", "v69", "v70", "v71", "v72", "v73", "v74", "v75", "v76", "v77", "v78", "v79", "v80", "v81", "v82", "v83", "v84", "v85", "v86", "v87", "v88", "v89", "v90", "v91", "v92", "v93", "v94", "v95", "v96", "v97", "v98", "v99", "v100", "v101", "v102", "v103", "v104", "v105", "v106", "v107", "v108", "v109", "v110", "v111", "v112", "v113", "v114", "v115", "v116", "v117", "v118", "v119", "v120", "v121", "v122", "v123", "v124", "v125", "v126", "v127", "v144", "v145", "v146", "v147", "v148", "v149", "v150", "v151", "v152", "v153", "v154", "v155", "v156", "v157", "v158", "v159", "v160", "v161", "v162", "v163", "v164", "v165", "v166", "v167", "v168", "v169", "v170", "v171", "v177", "v178", "v179", "v180", "v181", "v182", "v183", "v184"
    );
    const float l = o[9];
    bool ok = l > 8.67e-19f && l < 1.15e18f;
#pragma unroll
    for (int i = 0; i < 9; ++i) ok = ok && fabsf(o[i]) < 3.0e38f;
    if (!__all(ok)) o = attn_true_max(aK, aL, aV, qm, ql);
    const float inv = 1.f / o[9];
    const int t = qt * 32 + rho;
    u32x4 v;
#pragma unroll
    for (int j = 0; j < 4; ++j) v[j] = pk2(o[2 * j] * inv, o[2 * j + 1] * inv);
    *(u32x4*)(p.ws + WS_OM + (((size_t)(t >> 4) * 16 + (h >> 1)) * 64 + (t & 15) + 16 * (2 * (h & 1) + ha)) * 16) = v;
    const float mine = o[8] * inv, other = __shfl_xor(mine, 32);
    if (ha == 0) *(u32*)(p.ws + WS_OL + ((size_t)h * SEQ + t) * 4) = pk2(mine, other);
}

constexpr int WO_ROW = 160, WO_BYTES = 576 * WO_ROW, Y_OFF = WO_BYTES, RED4_OFF = Y_OFF + 72 * 128;
__device__ __forceinline__ int wo_row(int P) {
    const int st = P >> 5, q = P & 31;
    const int k = (q & 19) | ((q & 4) << 1) | ((q & 8) >> 1);
    const int g = k >> 3, j = k & 7;
    if (st < 16) return 36 * st + (g >> 1) * 20 + 8 * (g & 1) + j;
    return 36 * (8 * (st - 16) + 2 * g + (j >> 2)) + 16 + (j & 3);
}

__device__ __forceinline__ void phase_out(const Ptrs& p, int b, u32 lds) {
    const int tid = threadIdx.x, lane = tid & 63;
    const int w = uni(tid >> 6);
    const int xcd = b & 7, ii = b >> 3;
    const int tb = xcd * 4 + (ii >> 3), ng = ii & 7;
    const int tt = w & 3, kh = w >> 2;
    const int t16 = tb * 4 + tt, r = lane & 15, g = lane >> 4, t = t16 * 16 + r;
    for (int I = w; I < 90; I += 8) {
        const int idx = I * 64 + lane, P = idx / 10, c = idx - P * 10;
        dma16(p.wo, wo_row(P) * 1152 + ng * 144 + (c < 9 ? c : 0) * 16, lds + I * 1024);
    }
    for (int I = w; I < 9; I += 8) {
        const int idx = I * 64 + lane, n = idx >> 3, c = idx & 7;
        dma16(p.ws + WS_YT, (ng * 72 + n) * 4096 + tb * 128 + c * 16, lds + Y_OFF + I * 1024);
    }
    u32x4 of[9];
#pragma unroll
    for (int i = 0; i < 9; ++i) {
        const int st = 9 * kh + i;
        if (i < 7 || kh == 0) of[i] = *(const u32x4*)(p.ws + WS_OM + (((size_t)t16 * 16 + st) * 64 + lane) * 16);
        else {
#pragma unroll
            for (int j = 0; j < 4; ++j) of[i][j] = *(const u32*)(p.ws + WS_OL + ((size_t)(16 * (st - 16) + 4 * g + j) * SEQ + t) * 4);
        }
    }
    const float gate = ((const float*)(p.ws + WS_G))[t];
    u32x2 xv[3];
#pragma unroll
    for (int j = 0; j < 3; ++j) {
        const int nt = kh ? 3 + j : j, n = 16 * nt + 4 * g;
        xv[j] = (nt < 5 && n < 72) ? *(const u32x2*)(p.x + (size_t)t * DM + ng * 72 + n) : u32x2{0, 0};
    }
    WAITV(0);
    BAR();
    f32x4 acc[5];
#pragma unroll
    for (int n = 0; n < 5; ++n) acc[n] = f32x4{0.f, 0.f, 0.f, 0.f};
    const u32 wa = lds + (32 * 9 * kh + 16 * (g >> 1) + 4 * (g & 1) + (r >> 2)) * WO_ROW + (r & 3) * 8;
#pragma unroll
    for (int i = 0; i < 9; ++i)
#pragma unroll
        for (int n = 0; n < 5; ++n) {
            const u32 a = wa + i * 32 * WO_ROW + n * 32;
            acc[n] = __builtin_amdgcn_mfma_f32_16x16x32_bf16(cat8(lds_tr(a), lds_tr(a + 8 * WO_ROW)), __builtin_bit_cast(bf16x8, of[i]), acc[n], 0, 0, 0);
        }
    const u32 red = lds + RED4_OFF;
#pragma unroll
    for (int n = 0; n < 5; ++n) if ((n >= 3) != (kh == 1)) lds_st<f32x4>(red + (tt * 5 + n) * 1024 + lane * 16, acc[n]);
    BAR();
#pragma unroll
    for (int j = 0; j < 3; ++j) {
        const int nt = kh ? 3 + j : j, n = 16 * nt + 4 * g;
        if (nt < 5) {
            const f32x4 a = (kh ? acc[j < 2 ? 3 + j : 4] : acc[j]) + lds_ld<f32x4>(red + (tt * 5 + nt) * 1024 + lane * 16);
            const int yr = 16 * nt + 4 * g + (r >> 2);
            const s16x4 y = lds_tr(yr < 72 ? lds + Y_OFF + yr * 128 + tt * 32 + (r & 3) * 8 : OOR);
            if (n < 72) {
                float o[4];
#pragma unroll
                for (int e = 0; e < 4; ++e) {
                    const float xe = (e & 1) ? bfhi(xv[j][e >> 1]) : bflo(xv[j][e >> 1]);
                    const float ye = __uint_as_float((u32)(u16)y[e] << 16);
                    o[e] = gate * a[e] + (1.f - gate) * (xe * ye);
                }
                u32x2 v = {pk2(o[0], o[1]), pk2(o[2], o[3])};
                *(u32x2*)(p.out + (size_t)t * DM + ng * 72 + n) = v;
            }
        }
    }
}

constexpr int LDS1 = 4 * 32768 + 72 * 132 + 64;
constexpr int LDS2 = KVIMG + 8192;
constexpr int LDS3 = RED4_OFF + 20 * 1024;

__global__ __launch_bounds__(512, 2) void k1_qkv(Ptrs p) {
    __shared__ __attribute__((aligned(16))) char lds[LDS1];
    phase_prep(p, blockIdx.x, ldsaddr(lds) + 4 * 32768);
    phase_qkv(p, blockIdx.x, ldsaddr(lds));
}
__global__ __launch_bounds__(512, 2) void k2_attn(Ptrs p) {
    __shared__ __attribute__((aligned(16))) char lds[LDS2];
    phase_conv(p, blockIdx.x, ldsaddr(lds));
    phase_attn(p, blockIdx.x, ldsaddr(lds));
}
__global__ __launch_bounds__(512, 2) void k3_out(Ptrs p) {
    __shared__ __attribute__((aligned(16))) char lds[LDS3];
    phase_out(p, blockIdx.x, ldsaddr(lds));
}

extern "C" void kernel_launch(void* const* d_in, const int* in_sizes, int n_in, void* d_out, int out_size, void* d_ws, size_t ws_size,
                              hipStream_t stream) {
    Ptrs p;
    p.x = (const u16*)d_in[0]; p.wqkv = (const u16*)d_in[1]; p.wo = (const u16*)d_in[2];
    p.bk = (const u16*)d_in[4]; p.bv = (const u16*)d_in[5]; p.shk = (const u16*)d_in[6];
    p.ssmk = (const u16*)d_in[7]; p.ssmd = (const u16*)d_in[8]; p.gw = (const u16*)d_in[9];
    p.out = (u16*)d_out; p.ws = (char*)d_ws;
    k1_qkv<<<256, 512, 0, stream>>>(p);
    k2_attn<<<256, 512, 0, stream>>>(p);
    k3_out<<<256, 512, 0, stream>>>(p);
}
```

```cpp
#include <hip/hip_runtime.h>
#include <stdint.h>

typedef unsigned short u16;
typedef unsigned int u32;
typedef unsigned long long u64;
typedef float f32x4 __attribute__((ext_vector_type(4)));
typedef float f32x16 __attribute__((ext_vector_type(16)));
typedef u32 u32x2 __attribute__((ext_vector_type(2)));
typedef u32 u32x4 __attribute__((ext_vector_type(4)));
typedef u32 u32x8 __attribute__((ext_vector_type(8)));
typedef u32 u32x16 __attribute__((ext_vector_type(16)));
typedef short s16x4 __attribute__((ext_vector_type(4)));
typedef __bf16 bf16x2 __attribute__((ext_vector_type(2)));
typedef __bf16 bf16x4 __attribute__((ext_vector_type(4)));
typedef __bf16 bf16x8 __attribute__((ext_vector_type(8)));
typedef __bf16 bf16x16 __attribute__((ext_vector_type(16)));
typedef __amdgpu_buffer_rsrc_t rsrc_t;

constexpr int SEQ = 2048, DM = 576, NH = 32;
constexpr int CH = 9728;
constexpr int KVIMG = 16 * CH;
constexpr int VB = 640;
constexpr int UROW = 4224;
constexpr u32 WS_QM = 0;
constexpr u32 WS_QL = WS_QM + 2097152;
constexpr u32 WS_KV = WS_QL + 262144;
constexpr u32 WS_OM = WS_KV + NH * KVIMG;
constexpr u32 WS_OL = WS_OM + 2097152;
constexpr u32 WS_UT = WS_OL + 262144;
constexpr u32 WS_YT = WS_UT + DM * UROW;
constexpr u32 WS_G = WS_YT + DM * 4096;
constexpr u32 WS_F = WS_G + 8192;
constexpr u32 WS_END = WS_F + 24576;
constexpr u32 FL_F0 = 4096, FL_F1 = 8192, FL_F3 = 12288, FL_F3B = 16384;
constexpr u32 OOB = 0x7ffffff0u;
constexpr float QSCALE = 0.23570226039551584f * 1.4426950408889634f;
constexpr u32 OOR = 0x40000u;
constexpr int SC1 = 16;

__device__ __forceinline__ float bflo(u32 v) { return __uint_as_float(v << 16); }
__device__ __forceinline__ float bfhi(u32 v) { return __uint_as_float(v & 0xffff0000u); }
__device__ __forceinline__ u32 pk2(float lo, float hi) {
    typedef float f2 __attribute__((ext_vector_type(2)));
    f2 f = {lo, hi};
    return __builtin_bit_cast(u32, __builtin_convertvector(f, bf16x2));
}
__device__ __forceinline__ float dot2(u32 a, u32 b, float c) {
    return __builtin_amdgcn_fdot2_f32_bf16(__builtin_bit_cast(bf16x2, a), __builtin_bit_cast(bf16x2, b), c, false);
}
__device__ __forceinline__ u32 ldsaddr(const void* p) { return (u32)(uintptr_t)p; }
__device__ __forceinline__ u32 uni(u32 v) { return __builtin_amdgcn_readfirstlane(v); }
template <typename T> __device__ __forceinline__ T lds_ld(u32 a) { return *(const __attribute__((address_space(3))) T*)(uintptr_t)a; }
template <typename T> __device__ __forceinline__ void lds_st(u32 a, T v) { *(__attribute__((address_space(3))) T*)(uintptr_t)a = v; }
__device__ __forceinline__ s16x4 lds_tr(u32 a) {
    typedef short v4i16_t __attribute__((ext_vector_type(4)));
    return __builtin_amdgcn_ds_read_tr16_b64_v4i16((__attribute__((address_space(3))) v4i16_t*)(uintptr_t)a);
}
__device__ __forceinline__ bf16x8 cat8(s16x4 a, s16x4 b) {
    typedef short s8 __attribute__((ext_vector_type(8)));
    s8 r = {a[0], a[1], a[2], a[3], b[0], b[1], b[2], b[3]};
    return __builtin_bit_cast(bf16x8, r);
}
template <typename T> __device__ __forceinline__ const char* uni64(const T* p) {
    const uint64_t a = (uint64_t)p;
    return (const char*)(((uint64_t)uni((u32)(a >> 32)) << 32) | uni((u32)a));
}
__device__ __forceinline__ void st16(rsrc_t r, u32 off, u32x4 v) { __builtin_amdgcn_raw_buffer_store_b128(v, r, off, 0, SC1); }
__device__ __forceinline__ void st4(rsrc_t r, u32 off, u32 v) { __builtin_amdgcn_raw_buffer_store_b32(v, r, off, 0, SC1); }
__device__ __forceinline__ void st8(rsrc_t r, u32 off, u64 v) {
    u32x2 w = {(u32)v, (u32)(v >> 32)};
    __builtin_amdgcn_raw_buffer_store_b64(w, r, off, 0, SC1);
}
__device__ __forceinline__ u32x4 ld16(rsrc_t r, u32 off) { return __builtin_amdgcn_raw_buffer_load_b128(r, off, 0, SC1); }
__device__ __forceinline__ u32 ld4(rsrc_t r, u32 off) { return __builtin_amdgcn_raw_buffer_load_b32(r, off, 0, SC1); }
template <bool SC, bool CUT>
__device__ __forceinline__ void dma16x4(const char* gbase, u32 voff, u32 ldst) {
    u32 keep;
#define DMA4(S, LAST)                                                                                              \
    asm volatile("s_mov_b32 %0, m0\n\ts_mov_b32 m0, %3\n\ts_nop 0\n\t"                                           \
                 "global_load_lds_dwordx4 %1, %2" S "\n\tglobal_load_lds_dwordx4 %1, %2 offset:1024" S "\n\t"      \
                 "global_load_lds_dwordx4 %1, %2 offset:2048" S "\n\t" LAST "s_mov_b32 m0, %0"                     \
                 : "=&s"(keep) : "v"(voff), "s"(uni64(gbase)), "s"(uni(ldst)) : "memory", "scc")
    if (CUT) DMA4("", "s_lshr_b64 exec, exec, 1\n\tglobal_load_lds_dwordx4 %1, %2 offset:3072\n\ts_mov_b64 exec, -1\n\t");
    else if (SC) DMA4(" sc1", "global_load_lds_dwordx4 %1, %2 offset:3072 sc1\n\t");
    else DMA4("", "global_load_lds_dwordx4 %1, %2 offset:3072\n\t");
#undef DMA4
}
template <bool SC>
__device__ __forceinline__ void dma16(const void* gbase, u32 voff, u32 ldst) {
    u32 keep;
    if (SC)
        asm volatile("s_mov_b32 %0, m0\n\ts_mov_b32 m0, %3\n\ts_nop 0\n\tglobal_load_lds_dwordx4 %1, %2 sc1\n\ts_mov_b32 m0, %0"
                     : "=&s"(keep) : "v"(voff), "s"(uni64((const char*)gbase)), "s"(uni(ldst)) : "memory");
    else
        asm volatile("s_mov_b32 %0, m0\n\ts_mov_b32 m0, %3\n\ts_nop 0\n\tglobal_load_lds_dwordx4 %1, %2\n\ts_mov_b32 m0, %0"
                     : "=&s"(keep) : "v"(voff), "s"(uni64((const char*)gbase)), "s"(uni(ldst)) : "memory");
}
#define WAITV(n) asm volatile("s_waitcnt vmcnt(" #n ")" ::: "memory")
#define BAR() asm volatile("s_waitcnt lgkmcnt(0)\n\ts_barrier" ::: "memory")

__device__ __forceinline__ float wave_sum63(float x) {
#define DPP_ADD(ctrl, rm) x += __uint_as_float(__builtin_amdgcn_update_dpp(0u, __float_as_uint(x), ctrl, rm, 0xf, true))
    DPP_ADD(0x111, 0xf); DPP_ADD(0x112, 0xf); DPP_ADD(0x114, 0xf); DPP_ADD(0x118, 0xf); DPP_ADD(0x142, 0xa); DPP_ADD(0x143, 0xc);
#undef DPP_ADD
    return x;
}

__device__ __forceinline__ void wait_flags(const char* fbase, u32 off, u64 tag) {
#pragma unroll 1
    for (int it = 0; it < 40000; ++it) {
        u64 v;
        asm volatile("global_load_dwordx2 %0, %1, %2 sc1\n\ts_waitcnt vmcnt(0)" : "=v"(v) : "v"(off), "s"(fbase) : "memory");
        if (__all(v == tag)) break;
        __builtin_amdgcn_s_sleep(1);
    }
}


struct Ptrs {
    const u16* x; const u16* wqkv; const u16* wo; const u16* bk; const u16* bv;
    const u16* shk; const u16* ssmk; const u16* ssmd; const u16* gw;
    u16* out; char* ws;
};

constexpr int HC = 4160, UC = 4544;
constexpr int CONV_CH = 4 * HC + 2 * UC + 32;
constexpr u32 CONV_A = 124064, CONV_B = 98304, CONV_C = 65536, CONV_RED = 40960;
constexpr u32 LEFT_ST = 149824;
constexpr u32 QST = 152896;
constexpr u32 P0_A = 131072, P0_V = 10240, P0_W = 19456, P0_ST = 151552;
__device__ __forceinline__ u32 conv_area(int cc) { return cc == 0 ? CONV_A : (cc == 1 ? CONV_B : CONV_C); }
__device__ __forceinline__ int conv_chan(int b, int cc) { return 9 * (b >> 2) + (cc < 2 ? 2 * (b & 3) + cc : 8); }

struct Carry { u64 tag; u32 dv; u32 uda; u32x4 ua[2], ub[2]; };

__device__ __forceinline__ Carry phase_qkv(const Ptrs& p, rsrc_t ws, int b, u32 lds) {
    int lane = threadIdx.x & 63, w = uni(threadIdx.x >> 6);
    const int xcd = b & 7, ii = b >> 3;
    const int tb = (xcd >> 1) * 4 + (ii & 3), cg = (xcd & 1) * 8 + (ii >> 2);
    const int tblk = xcd * 4 + (ii >> 3), cg0 = ii & 7, t0 = tblk * 64, c0 = cg0 * 72;
    const char* fb = uni64(p.ws + WS_F);
    const char* xg = uni64((const char*)p.x + (size_t)tb * 128 * 1152);
    const char* wg = uni64((const char*)p.wqkv);
    const u32 lw = uni(lds + w * 2048);
    u32x4 xov, xav, pvv;
    u32x8 psv;
    u64 pex;
    u32 wa;
    int ty;
    {
        const int l4 = lane >> 4, c3 = lane & 3;
        const u32 xr = (16 * w + (lane >> 3)) * 1152, sw = (lane & 7) ^ l4;
        const u32 offm = 72 * l4 + 16 * c3 + 8 * (c3 >> 1), offl = 72 * (c3 < 2 ? c3 : 2) + 32;
        const u32 wo0 = (8 * w + ((lane >> 2) & 3)) * 3456 + 216 * cg + (l4 < 3 ? offm : offl);
        xov = u32x4{xr + (sw << 4), xr + 8 * 1152 + ((sw ^ 4) << 4) - 1024, wo0, wo0 + 4 * 3456 - 1024};
        asm volatile(
        "s_mov_b32 s21, m0\n\t"
        "s_mov_b64 s[22:23], %[xg]\n\t"
        "s_mov_b64 s[24:25], %[wg]\n\t"
        "s_mov_b32 s26, %[lw]\n\t"
        "s_add_u32 m0, s26, 0\n\t"
        "s_nop 0\n\t"
        "global_load_lds_dwordx4 v122, s[22:23]\n\t"
        "global_load_lds_dwordx4 v123, s[22:23] offset:1024\n\t"
        "s_add_u32 m0, s26, 16384\n\t"
        "s_nop 0\n\t"
        "global_load_lds_dwordx4 v124, s[24:25]\n\t"
        "global_load_lds_dwordx4 v125, s[24:25] offset:1024\n\t"
        "s_mov_b32 m0, s21\n\t"
            :
            : [xg] "s"(xg), [wg] "s"(wg), [lw] "s"(lw), "{v[122:125]}"(xov)
            : "s21", "s22", "s23", "s24", "s25", "s26", "scc", "memory");
        const int q = 64 * w + lane, rr = (q * 7282) >> 16;
        const u32 X = c0 * 2 + (q - 9 * rr) * 16;
        const int tk = t0 - 3 + rr;
        const u32 po0 = (tk < 0 ? 0 : tk) * 1152 + X, po1 = (t0 + rr) * 1152 + X;
        int q2 = (w == 1 ? 576 : 512) + lane;
        q2 = q2 < 603 ? q2 : 602;
        const int r2 = (q2 * 7282) >> 16;
        u32 po2 = ((w == 2 ? t0 : t0 - 3) + r2) * 1152 + c0 * 2 + (q2 - 9 * r2) * 16;
        const u32 pot = c0 * 8 + (lane < 35 ? lane : 35) * 16;
        po2 = w == 3 ? pot : (w > 3 ? po1 : po2);
        const u64 pbk = (u64)uni64(p.bk), pbv = (u64)uni64(p.bv), psk = (u64)uni64(p.shk);
        const u64 pb2 = w < 2 ? pbk : (w == 3 ? psk : pbv);
        const u32 pm0 = lds + P0_A + w * 1024;
        const u32 pm2 = lds + P0_A + (w > 3 ? P0_V + w * 1024 : (w == 3 ? P0_W : 8192 + (w & 1) * 1024 + (w >> 1) * P0_V));
        const bool first = t0 == 0 && w == 0;
        pex = ((u64)0xffffffffu << 32) | uni(first ? 0xf8000000u : 0xffffffffu);
        lds_st<u32x4>(first && lane < 27 ? pm0 + lane * 16 : OOR, u32x4{0, 0, 0, 0});
        psv = u32x8{(u32)pbk, (u32)(pbk >> 32), (u32)pbv, (u32)(pbv >> 32), uni((u32)pb2), uni((u32)(pb2 >> 32)), uni(pm0), uni(pm2)};
        pvv = u32x4{po0, po1, po2, 0};
        const int ft = w & 3, th = w >> 2;
        const int r = lane & 31, hh = lane >> 5;
        const int blk = 3 * cg + ft;
        ty = ft < 3 ? blk >> 4 : 0;
        const u32 base = lds + th * 8192 + r * 128, u = (hh ^ ((r >> 1) & 7)) << 4;
        xav = u32x4{base + u, base + (u ^ 32), base + (u ^ 64), base + (u ^ 96)};
        const u32 rowo = lds + 16384 + hh * 2048 + ((lane >> 2) & 3) * 64;
        const u32 mc = ty == 2 ? c3 : (((c3 & 1) << 1) | (c3 >> 1));
        const u32 wm = rowo + ft * 256 + (l4 & 1) * 32 + mc * 8, wl = ((l4 & 1) == 0 && c3 < 3) ? rowo + 768 + c3 * 16 : OOR;
        wa = ft == 3 ? wl : wm;
    }
    const int tid = w * 64 + lane;
    const int chA = 9 * (b >> 2) + 2 * (b & 3), ch2 = 9 * (b >> 2) + 8;
    const int ft = w & 3, th = w >> 2;
    const int l4 = lane >> 4, c3 = lane & 3, r = lane & 31, hh = lane >> 5, fhl = l4 & 1;
    const int blk = 3 * cg + ft, h0 = 2 * (blk & 15);
    u32x8 pa;
    u32x4 px;
    {
        const u32 tp = tid >> 3, pc = tid & 7;
        pa[0] = lds + P0_A + tp * 288 + pc * 16;
        pa[1] = lds + P0_A + P0_W + pc * 64;
        pa[2] = lds + P0_ST + pc * 1056 + (31 - tp) * 4;
        pa[3] = lds + P0_ST + tp * 132 + pc * 16;
        pa[4] = pa[3] + (w == 0 ? 64 * 132 : OOR);
        const u32 ub = WS_UT + c0 * UROW;
        pa[5] = ub + (1984 - t0) * 2 + tp * UROW + pc * 16;
        const u32 ch = tid - 64;
        const u32 pad = ch < 72 ? ub + 4096 + ch * UROW : OOB;
        pa[6] = w == 0 ? pa[5] + 64 * UROW : (t0 == 0 ? pad : OOB);
        pa[7] = FL_F0 + (cg0 * 32 + tblk) * 8;
        px = u32x4{lds + P0_A + 128 + 2 * w + lane * 144, lds + P0_A + P0_W + (64 + w) * 8, lds + P0_ST + (64 + w) * 132 + 126 - 2 * lane, 0};
    }
    u32 eo0;
    {
        const u32 kvb = WS_KV + h0 * KVIMG + tb * CH;
        const u32 sb = ty == 0 ? WS_QM + (h0 * 64 + tb * 4 + 2 * th) * 1024 : kvb + (ty == 1 ? 2 * th * 1024 : 4608 + 4 * th * VB);
        const u32 lv = fhl * KVIMG + hh * 256 + (lane & 15) * 16;
        eo0 = sb + (ty == 2 ? lv : lane * 16);
    }
    const u32 f0p = FL_F0 + ((b >> 5) * 32 + (lane & 31)) * 8;
    f32x16 acc[2];
    u32 uok;
    u32x8 misc;
    {
        const u32 hdo = (l4 < 2 ? chA + l4 : ch2) * 4096, dvo = (w < 4 ? chA + (w >> 1) : ch2) * 2;
        const u64 sk = (u64)uni64(p.ssmk);
        const u64 hb0 = sk + (u32)((chA + th) * 4096 + 4 * ft + 4);
        const u64 hb1 = sk + (u32)(ch2 * 4096 + 4 * (w >> 1) + 4 + (w & 1) * 2048);
        const u32 hm0 = lds + (w < 4 ? CONV_A : CONV_B) + ft * HC + 64, hm1 = lds + CONV_C + (w >> 1) * HC + 64 + (w & 1) * 2048;
        const u32x8 hsv = {uni((u32)hb0), uni((u32)(hb0 >> 32)), uni((u32)hb1), uni((u32)(hb1 >> 32)), uni(hm0), uni(hm1), 0xffffffffu,
                           uni((w & 1) ? 0x7fffffffu : 0xffffffffu)};
        const u64 wsa = (u64)p.ws;
        const u32x4 rs = {uni((u32)wsa), uni((u32)(wsa >> 32) & 0xffffu), WS_END, 0x00020000u};
        asm volatile("" : "+v"(eo0));
        asm volatile(
        "s_mov_b32 s21, m0\n\t"
        "s_mov_b64 s[22:23], %[xg]\n\t"
        "s_mov_b64 s[24:25], %[wg]\n\t"
        "s_mov_b32 s26, %[lw]\n\t"
        "s_add_u32 s22, s22, 128\n\t"
        "s_addc_u32 s23, s23, 0\n\t"
        "s_add_u32 s24, s24, 221184\n\t"
        "s_addc_u32 s25, s25, 0\n\t"
        "s_add_u32 m0, s26, 98304\n\t"
        "s_nop 0\n\t"
        "global_load_lds_dwordx4 v122, s[22:23]\n\t"
        "global_load_lds_dwordx4 v123, s[22:23] offset:1024\n\t"
        "s_add_u32 m0, s26, 114688\n\t"
        "s_nop 0\n\t"
        "global_load_lds_dwordx4 v124, s[24:25]\n\t"
        "global_load_lds_dwordx4 v125, s[24:25] offset:1024\n\t"
        "s_mov_b32 m0, s21\n\t"
            :
            : [xg] "s"(xg), [wg] "s"(wg), [lw] "s"(lw), "{v[122:125]}"(xov), "v"(pa), "v"(px), "v"(hdo), "v"(dvo), "v"(f0p), "s"(hsv)
            : "s21", "s22", "s23", "s24", "s25", "s26", "scc", "memory");
#define P1_INS                                                                                                                     \
            : [var] "s"(uni(var)), [xg] "s"(xg), [wg] "s"(wg), [lw] "s"(lw), "{v[122:125]}"(xov), "{v[112:115]}"(xav), "{v[184:191]}"(pa), \
              "{v[200:203]}"(px), [wa] "v"(wa), [wv] "s"(w), [epb] "s"(fb), [f0p] "v"(f0p), [l16] "v"(lane * 16), "{s[44:51]}"(hsv),    \
              [rs] "s"(rs), [hdo] "v"(hdo), [ssmk] "s"(sk), [dvo] "v"(dvo), [ssmd] "s"(uni64(p.ssmd)),                              \
              "{s[36:43]}"(psv), "{v[204:207]}"(pvv), [pex] "s"(pex)
        const int var = (ty == 2 ? 2 : 0) + (w < 4 ? 0 : 1);
        asm volatile(
        "s_cmp_eq_u32 %[var], 1\n\t"
        "s_cbranch_scc1 11f\n\t"
        "s_cmp_eq_u32 %[var], 2\n\t"
        "s_cbranch_scc1 12f\n\t"
        "s_cmp_eq_u32 %[var], 3\n\t"
        "s_cbranch_scc1 13f\n\t"
        "s_mov_b32 s21, m0\n\t"
        "s_mov_b64 s[22:23], %[xg]\n\t"
        "s_mov_b64 s[24:25], %[wg]\n\t"
        "s_mov_b32 s26, %[lw]\n\t"
        "s_add_u32 s22, s22, 256\n\t"
        "s_addc_u32 s23, s23, 0\n\t"
        "s_add_u32 s24, s24, 442368\n\t"
        "s_addc_u32 s25, s25, 0\n\t"
        "s_mov_b32 %[uok], 1\n\t"
        "s_mov_b32 s30, 0x05040100\n\t"
        "s_mov_b32 s31, 0x07060302\n\t"
        "v_mov_b32 v172, 0\n\t"
        "global_load_dwordx2 v[196:197], v172, %[epb] sc1\n\t"
        "global_load_dwordx4 v[192:195], %[hdo], %[ssmk]\n\t"
        "global_load_ushort v198, %[dvo], %[ssmd]\n\t"
        "v_add_u32 v116, 0x10000, v112\n\t"
        "v_add_u32 v117, 0x10000, v113\n\t"
        "v_add_u32 v118, 0x10000, v114\n\t"
        "v_add_u32 v119, 0x10000, v115\n\t"
        "v_mov_b32 v120, %[wa]\n\t"
        "v_add_u32 v121, 0x10000, %[wa]\n\t"
        "v_mov_b32 v32, 0\n\t"
        "v_mov_b32 v33, 0\n\t"
        "v_mov_b32 v34, 0\n\t"
        "v_mov_b32 v35, 0\n\t"
        "v_mov_b32 v36, 0\n\t"
        "v_mov_b32 v37, 0\n\t"
        "v_mov_b32 v38, 0\n\t"
        "v_mov_b32 v39, 0\n\t"
        "v_mov_b32 v40, 0\n\t"
        "v_mov_b32 v41, 0\n\t"
        "v_mov_b32 v42, 0\n\t"
        "v_mov_b32 v43, 0\n\t"
        "v_mov_b32 v44, 0\n\t"
        "v_mov_b32 v45, 0\n\t"
        "v_mov_b32 v46, 0\n\t"
        "v_mov_b32 v47, 0\n\t"
        "v_mov_b32 v48, 0\n\t"
        "v_mov_b32 v49, 0\n\t"
        "v_mov_b32 v50, 0\n\t"
        "v_mov_b32 v51, 0\n\t"
        "v_mov_b32 v52, 0\n\t"
        "v_mov_b32 v53, 0\n\t"
        "v_mov_b32 v54, 0\n\t"
        "v_mov_b32 v55, 0\n\t"
        "v_mov_b32 v56, 0\n\t"
        "v_mov_b32 v57, 0\n\t"
        "v_mov_b32 v58, 0\n\t"
        "v_mov_b32 v59, 0\n\t"
        "v_mov_b32 v60, 0\n\t"
        "v_mov_b32 v61, 0\n\t"
        "v_mov_b32 v62, 0\n\t"
        "v_mov_b32 v63, 0\n\t"
        "v_mov_b32 v208, 0\n\t"
        "v_mov_b32 v209, 0\n\t"
        "v_mov_b32 v210, 0\n\t"
        "v_mov_b32 v211, 0\n\t"
        "v_mov_b32 v212, 0\n\t"
        "v_mov_b32 v213, 0\n\t"
        "v_mov_b32 v214, 0\n\t"
        "v_mov_b32 v215, 0\n\t"
        "v_mov_b32 v216, 0\n\t"
        "s_waitcnt vmcnt(7)\n\t"
        "s_barrier\n\t"
        "ds_read_b64_tr_b16 v[64:65], v120 offset:0\n\t"
        "ds_read_b64_tr_b16 v[66:67], v120 offset:1024\n\t"
        "ds_read_b128 v[68:71], v112 offset:0\n\t"
        "ds_read_b128 v[72:75], v112 offset:4096\n\t"
        "ds_read_b64_tr_b16 v[76:77], v120 offset:4096\n\t"
        "ds_read_b64_tr_b16 v[78:79], v120 offset:5120\n\t"
        "ds_read_b128 v[80:83], v113 offset:0\n\t"
        "ds_read_b128 v[84:87], v113 offset:4096\n\t"
        "s_mov_b32 m0, s42\n\t"
        "s_mov_b64 exec, %[pex]\n\t"
        "global_load_lds_dwordx4 v204, s[36:37]\n\t"
        "s_mov_b64 exec, -1\n\t"
        "s_add_u32 m0, s42, 10240\n\t"
        "s_nop 0\n\t"
        "global_load_lds_dwordx4 v205, s[38:39]\n\t"
        "s_mov_b32 m0, s43\n\t"
        "s_nop 0\n\t"
        "global_load_lds_dwordx4 v206, s[40:41]\n\t"
        "s_add_u32 m0, s26, 65536\n\t"
        "s_nop 0\n\t"
        "global_load_lds_dwordx4 v122, s[22:23]\n\t"
        "global_load_lds_dwordx4 v123, s[22:23] offset:1024\n\t"
        "s_add_u32 m0, s26, 81920\n\t"
        "s_nop 0\n\t"
        "global_load_lds_dwordx4 v124, s[24:25]\n\t"
        "global_load_lds_dwordx4 v125, s[24:25] offset:1024\n\t"
        "s_add_u32 s22, s22, 128\n\t"
        "s_addc_u32 s23, s23, 0\n\t"
        "s_add_u32 s24, s24, 221184\n\t"
        "s_addc_u32 s25, s25, 0\n\t"
        "s_waitcnt lgkmcnt(5)\n\t"
        "v_mfma_f32_32x32x16_bf16 v[32:47], v[64:67], v[68:71], v[32:47]\n\t"
        "s_waitcnt lgkmcnt(4)\n\t"
        "v_mfma_f32_32x32x16_bf16 v[48:63], v[64:67], v[72:75], v[48:63]\n\t"
        "ds_read_b64_tr_b16 v[88:89], v120 offset:8192\n\t"
        "ds_read_b64_tr_b16 v[90:91], v120 offset:9216\n\t"
        "ds_read_b128 v[92:95], v114 offset:0\n\t"
        "ds_read_b128 v[96:99], v114 offset:4096\n\t"
        "s_waitcnt lgkmcnt(5)\n\t"
        "v_mfma_f32_32x32x16_bf16 v[32:47], v[76:79], v[80:83], v[32:47]\n\t"
        "s_waitcnt lgkmcnt(4)\n\t"
        "v_mfma_f32_32x32x16_bf16 v[48:63], v[76:79], v[84:87], v[48:63]\n\t"
        "s_waitcnt vmcnt(10)\n\t"
        "s_barrier\n\t"
        "s_add_u32 m0, s26, 32768\n\t"
        "s_nop 0\n\t"
        "global_load_lds_dwordx4 v122, s[22:23]\n\t"
        "global_load_lds_dwordx4 v123, s[22:23] offset:1024\n\t"
        "s_add_u32 m0, s26, 49152\n\t"
        "s_nop 0\n\t"
        "global_load_lds_dwordx4 v124, s[24:25]\n\t"
        "global_load_lds_dwordx4 v125, s[24:25] offset:1024\n\t"
        "s_add_u32 s22, s22, 128\n\t"
        "s_addc_u32 s23, s23, 0\n\t"
        "s_add_u32 s24, s24, 221184\n\t"
        "s_addc_u32 s25, s25, 0\n\t"
        "ds_read_b64_tr_b16 v[100:101], v120 offset:12288\n\t"
        "ds_read_b64_tr_b16 v[102:103], v120 offset:13312\n\t"
        "ds_read_b128 v[104:107], v115 offset:0\n\t"
        "ds_read_b128 v[108:111], v115 offset:4096\n\t"
        "s_waitcnt lgkmcnt(5)\n\t"
        "v_mfma_f32_32x32x16_bf16 v[32:47], v[88:91], v[92:95], v[32:47]\n\t"
        "s_waitcnt lgkmcnt(4)\n\t"
        "v_mfma_f32_32x32x16_bf16 v[48:63], v[88:91], v[96:99], v[48:63]\n\t"
        "ds_read_b64_tr_b16 v[64:65], v121 offset:32768\n\t"
        "ds_read_b64_tr_b16 v[66:67], v121 offset:33792\n\t"
        "ds_read_b128 v[68:71], v116 offset:32768\n\t"
        "ds_read_b128 v[72:75], v116 offset:36864\n\t"
        "s_waitcnt lgkmcnt(5)\n\t"
        "v_mfma_f32_32x32x16_bf16 v[32:47], v[100:103], v[104:107], v[32:47]\n\t"
        "s_waitcnt lgkmcnt(4)\n\t"
        "v_mfma_f32_32x32x16_bf16 v[48:63], v[100:103], v[108:111], v[48:63]\n\t"
        "ds_read_b64_tr_b16 v[76:77], v121 offset:36864\n\t"
        "ds_read_b64_tr_b16 v[78:79], v121 offset:37888\n\t"
        "ds_read_b128 v[80:83], v117 offset:32768\n\t"
        "ds_read_b128 v[84:87], v117 offset:36864\n\t"
        "s_waitcnt lgkmcnt(5)\n\t"
        "v_mfma_f32_32x32x16_bf16 v[32:47], v[64:67], v[68:71], v[32:47]\n\t"
        "s_waitcnt lgkmcnt(4)\n\t"
        "v_mfma_f32_32x32x16_bf16 v[48:63], v[64:67], v[72:75], v[48:63]\n\t"
        "ds_read_b64_tr_b16 v[88:89], v121 offset:40960\n\t"
        "ds_read_b64_tr_b16 v[90:91], v121 offset:41984\n\t"
        "ds_read_b128 v[92:95], v118 offset:32768\n\t"
        "ds_read_b128 v[96:99], v118 offset:36864\n\t"
        "s_waitcnt lgkmcnt(5)\n\t"
        "v_mfma_f32_32x32x16_bf16 v[32:47], v[76:79], v[80:83], v[32:47]\n\t"
        "s_waitcnt lgkmcnt(4)\n\t"
        "v_mfma_f32_32x32x16_bf16 v[48:63], v[76:79], v[84:87], v[48:63]\n\t"
        "s_waitcnt vmcnt(4)\n\t"
        "s_barrier\n\t"
        "s_add_u32 m0, s26, 0\n\t"
        "s_nop 0\n\t"
        "global_load_lds_dwordx4 v122, s[22:23]\n\t"
        "global_load_lds_dwordx4 v123, s[22:23] offset:1024\n\t"
        "s_add_u32 m0, s26, 16384\n\t"
        "s_nop 0\n\t"
        "global_load_lds_dwordx4 v124, s[24:25]\n\t"
        "global_load_lds_dwordx4 v125, s[24:25] offset:1024\n\t"
        "s_add_u32 s22, s22, 128\n\t"
        "s_addc_u32 s23, s23, 0\n\t"
        "s_add_u32 s24, s24, 221184\n\t"
        "s_addc_u32 s25, s25, 0\n\t"
        "ds_read_b64_tr_b16 v[100:101], v121 offset:45056\n\t"
        "ds_read_b64_tr_b16 v[102:103], v121 offset:46080\n\t"
        "ds_read_b128 v[104:107], v119 offset:32768\n\t"
        "ds_read_b128 v[108:111], v119 offset:36864\n\t"
        "s_waitcnt lgkmcnt(5)\n\t"
        "v_mfma_f32_32x32x16_bf16 v[32:47], v[88:91], v[92:95], v[32:47]\n\t"
        "s_waitcnt lgkmcnt(4)\n\t"
        "v_mfma_f32_32x32x16_bf16 v[48:63], v[88:91], v[96:99], v[48:63]\n\t"
        "ds_read_b64_tr_b16 v[64:65], v121 offset:0\n\t"
        "ds_read_b64_tr_b16 v[66:67], v121 offset:1024\n\t"
        "ds_read_b128 v[68:71], v116 offset:0\n\t"
        "ds_read_b128 v[72:75], v116 offset:4096\n\t"
        "ds_read_b128 v[128:131], v184 offset:0\n\t"
        "ds_read_b128 v[132:135], v184 offset:144\n\t"
        "ds_read_b128 v[136:139], v184 offset:288\n\t"
        "ds_read_b128 v[140:143], v184 offset:432\n\t"
        "ds_read_b128 v[144:147], v184 offset:576\n\t"
        "s_waitcnt lgkmcnt(10)\n\t"
        "v_mfma_f32_32x32x16_bf16 v[32:47], v[100:103], v[104:107], v[32:47]\n\t"
        "s_waitcnt lgkmcnt(9)\n\t"
        "v_mfma_f32_32x32x16_bf16 v[48:63], v[100:103], v[108:111], v[48:63]\n\t"
        "ds_read_b64_tr_b16 v[76:77], v121 offset:4096\n\t"
        "ds_read_b64_tr_b16 v[78:79], v121 offset:5120\n\t"
        "ds_read_b128 v[80:83], v117 offset:0\n\t"
        "ds_read_b128 v[84:87], v117 offset:4096\n\t"
        "ds_read_b128 v[148:151], v184 offset:10240\n\t"
        "ds_read_b128 v[152:155], v184 offset:10384\n\t"
        "ds_read_b128 v[156:159], v185 offset:0\n\t"
        "ds_read_b128 v[160:163], v185 offset:16\n\t"
        "ds_read_b128 v[164:167], v185 offset:32\n\t"
        "ds_read_b128 v[168:171], v185 offset:48\n\t"
        "s_waitcnt lgkmcnt(0)\n\t"
        "v_mfma_f32_32x32x16_bf16 v[32:47], v[64:67], v[68:71], v[32:47]\n\t"
        "v_perm_b32 v172, v128, v132, s30\n\t"
        "v_perm_b32 v178, v128, v132, s31\n\t"
        "v_perm_b32 v173, v132, v136, s30\n\t"
        "v_perm_b32 v179, v132, v136, s31\n\t"
        "v_perm_b32 v174, v136, v140, s30\n\t"
        "v_perm_b32 v180, v136, v140, s31\n\t"
        "v_perm_b32 v175, v140, v144, s30\n\t"
        "v_perm_b32 v181, v140, v144, s31\n\t"
        "v_dot2_f32_bf16 v176, v172, v157, 0\n\t"
        "v_dot2_f32_bf16 v182, v178, v159, 0\n\t"
        "v_dot2_f32_bf16 v177, v173, v157, 0\n\t"
        "v_dot2_f32_bf16 v183, v179, v159, 0\n\t"
        "v_dot2_f32_bf16 v176, v174, v156, v176\n\t"
        "v_dot2_f32_bf16 v182, v180, v158, v182\n\t"
        "v_mfma_f32_32x32x16_bf16 v[48:63], v[64:67], v[72:75], v[48:63]\n\t"
        "v_dot2_f32_bf16 v177, v175, v156, v177\n\t"
        "v_dot2_f32_bf16 v183, v181, v158, v183\n\t"
        "v_lshlrev_b32 v172, 16, v148\n\t"
        "v_and_b32 v178, 0xffff0000, v148\n\t"
        "v_lshlrev_b32 v173, 16, v152\n\t"
        "v_and_b32 v179, 0xffff0000, v152\n\t"
        "v_mul_f32 v174, v176, v172\n\t"
        "v_mul_f32 v180, v182, v178\n\t"
        "v_mul_f32 v175, v177, v173\n\t"
        "v_mul_f32 v181, v183, v179\n\t"
        "v_cvt_pk_bf16_f32 v174, v175, v174\n\t"
        "v_cvt_pk_bf16_f32 v180, v181, v180\n\t"
        "ds_write_b32 v186, v174 offset:0\n\t"
        "ds_write_b32 v186, v180 offset:132\n\t"
        "ds_read_b64_tr_b16 v[88:89], v121 offset:8192\n\t"
        "ds_read_b64_tr_b16 v[90:91], v121 offset:9216\n\t"
        "ds_read_b128 v[92:95], v118 offset:0\n\t"
        "ds_read_b128 v[96:99], v118 offset:4096\n\t"
        "v_mfma_f32_32x32x16_bf16 v[32:47], v[76:79], v[80:83], v[32:47]\n\t"
        "v_perm_b32 v172, v129, v133, s30\n\t"
        "v_perm_b32 v178, v129, v133, s31\n\t"
        "v_perm_b32 v173, v133, v137, s30\n\t"
        "v_perm_b32 v179, v133, v137, s31\n\t"
        "v_perm_b32 v174, v137, v141, s30\n\t"
        "v_perm_b32 v180, v137, v141, s31\n\t"
        "v_perm_b32 v175, v141, v145, s30\n\t"
        "v_perm_b32 v181, v141, v145, s31\n\t"
        "v_dot2_f32_bf16 v176, v172, v161, 0\n\t"
        "v_dot2_f32_bf16 v182, v178, v163, 0\n\t"
        "v_dot2_f32_bf16 v177, v173, v161, 0\n\t"
        "v_dot2_f32_bf16 v183, v179, v163, 0\n\t"
        "v_dot2_f32_bf16 v176, v174, v160, v176\n\t"
        "v_dot2_f32_bf16 v182, v180, v162, v182\n\t"
        "v_mfma_f32_32x32x16_bf16 v[48:63], v[76:79], v[84:87], v[48:63]\n\t"
        "v_dot2_f32_bf16 v177, v175, v160, v177\n\t"
        "v_dot2_f32_bf16 v183, v181, v162, v183\n\t"
        "v_lshlrev_b32 v172, 16, v149\n\t"
        "v_and_b32 v178, 0xffff0000, v149\n\t"
        "v_lshlrev_b32 v173, 16, v153\n\t"
        "v_and_b32 v179, 0xffff0000, v153\n\t"
        "v_mul_f32 v174, v176, v172\n\t"
        "v_mul_f32 v180, v182, v178\n\t"
        "v_mul_f32 v175, v177, v173\n\t"
        "v_mul_f32 v181, v183, v179\n\t"
        "v_cvt_pk_bf16_f32 v174, v175, v174\n\t"
        "v_cvt_pk_bf16_f32 v180, v181, v180\n\t"
        "ds_write_b32 v186, v174 offset:264\n\t"
        "ds_write_b32 v186, v180 offset:396\n\t"
        "s_waitcnt vmcnt(4)\n\t"
        "s_barrier\n\t"
        "s_add_u32 m0, s26, 98304\n\t"
        "s_nop 0\n\t"
        "global_load_lds_dwordx4 v122, s[22:23]\n\t"
        "global_load_lds_dwordx4 v123, s[22:23] offset:1024\n\t"
        "s_add_u32 m0, s26, 114688\n\t"
        "s_nop 0\n\t"
        "global_load_lds_dwordx4 v124, s[24:25]\n\t"
        "global_load_lds_dwordx4 v125, s[24:25] offset:1024\n\t"
        "s_add_u32 s22, s22, 128\n\t"
        "s_addc_u32 s23, s23, 0\n\t"
        "s_add_u32 s24, s24, 221184\n\t"
        "s_addc_u32 s25, s25, 0\n\t"
        "ds_read_b64_tr_b16 v[100:101], v121 offset:12288\n\t"
        "ds_read_b64_tr_b16 v[102:103], v121 offset:13312\n\t"
        "ds_read_b128 v[104:107], v119 offset:0\n\t"
        "ds_read_b128 v[108:111], v119 offset:4096\n\t"
        "s_waitcnt lgkmcnt(7)\n\t"
        "v_mfma_f32_32x32x16_bf16 v[32:47], v[88:91], v[92:95], v[32:47]\n\t"
        "v_perm_b32 v172, v130, v134, s30\n\t"
        "v_perm_b32 v178, v130, v134, s31\n\t"
        "v_perm_b32 v173, v134, v138, s30\n\t"
        "v_perm_b32 v179, v134, v138, s31\n\t"
        "v_perm_b32 v174, v138, v142, s30\n\t"
        "v_perm_b32 v180, v138, v142, s31\n\t"
        "v_perm_b32 v175, v142, v146, s30\n\t"
        "v_perm_b32 v181, v142, v146, s31\n\t"
        "v_dot2_f32_bf16 v176, v172, v165, 0\n\t"
        "v_dot2_f32_bf16 v182, v178, v167, 0\n\t"
        "v_dot2_f32_bf16 v177, v173, v165, 0\n\t"
        "v_dot2_f32_bf16 v183, v179, v167, 0\n\t"
        "v_dot2_f32_bf16 v176, v174, v164, v176\n\t"
        "v_dot2_f32_bf16 v182, v180, v166, v182\n\t"
        "s_waitcnt lgkmcnt(6)\n\t"
        "v_mfma_f32_32x32x16_bf16 v[48:63], v[88:91], v[96:99], v[48:63]\n\t"
        "v_dot2_f32_bf16 v177, v175, v164, v177\n\t"
        "v_dot2_f32_bf16 v183, v181, v166, v183\n\t"
        "v_lshlrev_b32 v172, 16, v150\n\t"
        "v_and_b32 v178, 0xffff0000, v150\n\t"
        "v_lshlrev_b32 v173, 16, v154\n\t"
        "v_and_b32 v179, 0xffff0000, v154\n\t"
        "v_mul_f32 v174, v176, v172\n\t"
        "v_mul_f32 v180, v182, v178\n\t"
        "v_mul_f32 v175, v177, v173\n\t"
        "v_mul_f32 v181, v183, v179\n\t"
        "v_cvt_pk_bf16_f32 v174, v175, v174\n\t"
        "v_cvt_pk_bf16_f32 v180, v181, v180\n\t"
        "ds_write_b32 v186, v174 offset:528\n\t"
        "ds_write_b32 v186, v180 offset:660\n\t"
        "ds_read_b64_tr_b16 v[64:65], v120 offset:32768\n\t"
        "ds_read_b64_tr_b16 v[66:67], v120 offset:33792\n\t"
        "ds_read_b128 v[68:71], v112 offset:32768\n\t"
        "ds_read_b128 v[72:75], v112 offset:36864\n\t"
        "s_waitcnt lgkmcnt(7)\n\t"
        "v_mfma_f32_32x32x16_bf16 v[32:47], v[100:103], v[104:107], v[32:47]\n\t"
        "v_perm_b32 v172, v131, v135, s30\n\t"
        "v_perm_b32 v178, v131, v135, s31\n\t"
        "v_perm_b32 v173, v135, v139, s30\n\t"
        "v_perm_b32 v179, v135, v139, s31\n\t"
        "v_perm_b32 v174, v139, v143, s30\n\t"
        "v_perm_b32 v180, v139, v143, s31\n\t"
        "v_perm_b32 v175, v143, v147, s30\n\t"
        "v_perm_b32 v181, v143, v147, s31\n\t"
        "v_dot2_f32_bf16 v176, v172, v169, 0\n\t"
        "v_dot2_f32_bf16 v182, v178, v171, 0\n\t"
        "v_dot2_f32_bf16 v177, v173, v169, 0\n\t"
        "v_dot2_f32_bf16 v183, v179, v171, 0\n\t"
        "v_dot2_f32_bf16 v176, v174, v168, v176\n\t"
        "v_dot2_f32_bf16 v182, v180, v170, v182\n\t"
        "s_waitcnt lgkmcnt(6)\n\t"
        "v_mfma_f32_32x32x16_bf16 v[48:63], v[100:103], v[108:111], v[48:63]\n\t"
        "v_dot2_f32_bf16 v177, v175, v168, v177\n\t"
        "v_dot2_f32_bf16 v183, v181, v170, v183\n\t"
        "v_lshlrev_b32 v172, 16, v151\n\t"
        "v_and_b32 v178, 0xffff0000, v151\n\t"
        "v_lshlrev_b32 v173, 16, v155\n\t"
        "v_and_b32 v179, 0xffff0000, v155\n\t"
        "v_mul_f32 v174, v176, v172\n\t"
        "v_mul_f32 v180, v182, v178\n\t"
        "v_mul_f32 v175, v177, v173\n\t"
        "v_mul_f32 v181, v183, v179\n\t"
        "v_cvt_pk_bf16_f32 v174, v175, v174\n\t"
        "v_cvt_pk_bf16_f32 v180, v181, v180\n\t"
        "ds_write_b32 v186, v174 offset:792\n\t"
        "ds_write_b32 v186, v180 offset:924\n\t"
        "ds_read_b64_tr_b16 v[76:77], v120 offset:36864\n\t"
        "ds_read_b64_tr_b16 v[78:79], v120 offset:37888\n\t"
        "ds_read_b128 v[80:83], v113 offset:32768\n\t"
        "ds_read_b128 v[84:87], v113 offset:36864\n\t"
        "ds_read_u16_d16_hi v208, v200 offset:0\n\t"
        "ds_read_u16_d16_hi v209, v200 offset:144\n\t"
        "ds_read_u16_d16_hi v210, v200 offset:288\n\t"
        "ds_read_u16_d16_hi v211, v200 offset:432\n\t"
        "ds_read_u16_d16_hi v212, v200 offset:10240\n\t"
        "ds_read_u16_d16_hi v213, v201 offset:6\n\t"
        "ds_read_u16_d16_hi v214, v201 offset:4\n\t"
        "ds_read_u16_d16_hi v215, v201 offset:2\n\t"
        "ds_read_u16_d16_hi v216, v201 offset:0\n\t"
        "s_waitcnt lgkmcnt(15)\n\t"
        "v_mfma_f32_32x32x16_bf16 v[32:47], v[64:67], v[68:71], v[32:47]\n\t"
        "s_waitcnt lgkmcnt(15)\n\t"
        "v_mfma_f32_32x32x16_bf16 v[48:63], v[64:67], v[72:75], v[48:63]\n\t"
        "s_waitcnt lgkmcnt(0)\n\t"
        "v_mul_f32 v213, v208, v213\n\t"
        "v_fmac_f32 v213, v209, v214\n\t"
        "v_fmac_f32 v213, v210, v215\n\t"
        "v_fmac_f32 v213, v211, v216\n\t"
        "v_mul_f32 v213, v213, v212\n\t"
        "v_cvt_pk_bf16_f32 v213, v213, v213\n\t"
        "ds_write_b16 v202, v213\n\t"
        "ds_read_b64_tr_b16 v[88:89], v120 offset:40960\n\t"
        "ds_read_b64_tr_b16 v[90:91], v120 offset:41984\n\t"
        "ds_read_b128 v[92:95], v114 offset:32768\n\t"
        "ds_read_b128 v[96:99], v114 offset:36864\n\t"
        "v_mfma_f32_32x32x16_bf16 v[32:47], v[76:79], v[80:83], v[32:47]\n\t"
        "v_mfma_f32_32x32x16_bf16 v[48:63], v[76:79], v[84:87], v[48:63]\n\t"
        "s_waitcnt vmcnt(4) lgkmcnt(4)\n\t"
        "s_barrier\n\t"
        "v_add_co_u32 v196, vcc, 1, v196\n\t"
        "s_nop 1\n\t"
        "v_addc_co_u32 v197, vcc, 0, v197, vcc\n\t"
        "s_add_u32 m0, s26, 65536\n\t"
        "s_nop 0\n\t"
        "global_load_lds_dwordx4 v122, s[22:23]\n\t"
        "global_load_lds_dwordx4 v123, s[22:23] offset:1024\n\t"
        "s_add_u32 m0, s26, 81920\n\t"
        "s_nop 0\n\t"
        "global_load_lds_dwordx4 v124, s[24:25]\n\t"
        "global_load_lds_dwordx4 v125, s[24:25] offset:1024\n\t"
        "s_add_u32 s22, s22, 128\n\t"
        "s_addc_u32 s23, s23, 0\n\t"
        "s_add_u32 s24, s24, 221184\n\t"
        "s_addc_u32 s25, s25, 0\n\t"
        "ds_read_b64_tr_b16 v[100:101], v120 offset:45056\n\t"
        "ds_read_b64_tr_b16 v[102:103], v120 offset:46080\n\t"
        "ds_read_b128 v[104:107], v115 offset:32768\n\t"
        "ds_read_b128 v[108:111], v115 offset:36864\n\t"
        "s_waitcnt lgkmcnt(5)\n\t"
        "v_mfma_f32_32x32x16_bf16 v[32:47], v[88:91], v[92:95], v[32:47]\n\t"
        "s_waitcnt lgkmcnt(4)\n\t"
        "v_mfma_f32_32x32x16_bf16 v[48:63], v[88:91], v[96:99], v[48:63]\n\t"
        "ds_read_b64_tr_b16 v[64:65], v120 offset:0\n\t"
        "ds_read_b64_tr_b16 v[66:67], v120 offset:1024\n\t"
        "ds_read_b128 v[68:71], v112 offset:0\n\t"
        "ds_read_b128 v[72:75], v112 offset:4096\n\t"
        "ds_read2_b32 v[128:129], v187 offset0:0 offset1:1\n\t"
        "ds_read2_b32 v[130:131], v187 offset0:2 offset1:3\n\t"
        "ds_read2_b32 v[132:133], v188 offset0:0 offset1:1\n\t"
        "ds_read2_b32 v[134:135], v188 offset0:2 offset1:3\n\t"
        "s_waitcnt lgkmcnt(9)\n\t"
        "v_mfma_f32_32x32x16_bf16 v[32:47], v[100:103], v[104:107], v[32:47]\n\t"
        "s_waitcnt lgkmcnt(8)\n\t"
        "v_mfma_f32_32x32x16_bf16 v[48:63], v[100:103], v[108:111], v[48:63]\n\t"
        "s_waitcnt lgkmcnt(0)\n\t"
        "buffer_store_dwordx4 v[128:131], v189, %[rs], 0 offen sc1\n\t"
        "buffer_store_dwordx4 v[132:135], v190, %[rs], 0 offen sc1\n\t"
        "ds_read_b64_tr_b16 v[76:77], v120 offset:4096\n\t"
        "ds_read_b64_tr_b16 v[78:79], v120 offset:5120\n\t"
        "ds_read_b128 v[80:83], v113 offset:0\n\t"
        "ds_read_b128 v[84:87], v113 offset:4096\n\t"
        "v_mfma_f32_32x32x16_bf16 v[32:47], v[64:67], v[68:71], v[32:47]\n\t"
        "v_mfma_f32_32x32x16_bf16 v[48:63], v[64:67], v[72:75], v[48:63]\n\t"
        "ds_read_b64_tr_b16 v[88:89], v120 offset:8192\n\t"
        "ds_read_b64_tr_b16 v[90:91], v120 offset:9216\n\t"
        "ds_read_b128 v[92:95], v114 offset:0\n\t"
        "ds_read_b128 v[96:99], v114 offset:4096\n\t"
        "s_waitcnt lgkmcnt(5)\n\t"
        "v_mfma_f32_32x32x16_bf16 v[32:47], v[76:79], v[80:83], v[32:47]\n\t"
        "s_waitcnt lgkmcnt(4)\n\t"
        "v_mfma_f32_32x32x16_bf16 v[48:63], v[76:79], v[84:87], v[48:63]\n\t"
        "s_waitcnt vmcnt(6)\n\t"
        "s_barrier\n\t"
        "s_add_u32 m0, s26, 32768\n\t"
        "s_nop 0\n\t"
        "global_load_lds_dwordx4 v122, s[22:23]\n\t"
        "global_load_lds_dwordx4 v123, s[22:23] offset:1024\n\t"
        "s_add_u32 m0, s26, 49152\n\t"
        "s_nop 0\n\t"
        "global_load_lds_dwordx4 v124, s[24:25]\n\t"
        "global_load_lds_dwordx4 v125, s[24:25] offset:1024\n\t"
        "s_add_u32 s22, s22, 128\n\t"
        "s_addc_u32 s23, s23, 0\n\t"
        "s_add_u32 s24, s24, 221184\n\t"
        "s_addc_u32 s25, s25, 0\n\t"
        "ds_read_b64_tr_b16 v[100:101], v120 offset:12288\n\t"
        "ds_read_b64_tr_b16 v[102:103], v120 offset:13312\n\t"
        "ds_read_b128 v[104:107], v115 offset:0\n\t"
        "ds_read_b128 v[108:111], v115 offset:4096\n\t"
        "s_waitcnt lgkmcnt(5)\n\t"
        "v_mfma_f32_32x32x16_bf16 v[32:47], v[88:91], v[92:95], v[32:47]\n\t"
        "s_waitcnt lgkmcnt(4)\n\t"
        "v_mfma_f32_32x32x16_bf16 v[48:63], v[88:91], v[96:99], v[48:63]\n\t"
        "ds_read_b64_tr_b16 v[64:65], v121 offset:32768\n\t"
        "ds_read_b64_tr_b16 v[66:67], v121 offset:33792\n\t"
        "ds_read_b128 v[68:71], v116 offset:32768\n\t"
        "ds_read_b128 v[72:75], v116 offset:36864\n\t"
        "s_waitcnt lgkmcnt(5)\n\t"
        "v_mfma_f32_32x32x16_bf16 v[32:47], v[100:103], v[104:107], v[32:47]\n\t"
        "s_waitcnt lgkmcnt(4)\n\t"
        "v_mfma_f32_32x32x16_bf16 v[48:63], v[100:103], v[108:111], v[48:63]\n\t"
        "ds_read_b64_tr_b16 v[76:77], v121 offset:36864\n\t"
        "ds_read_b64_tr_b16 v[78:79], v121 offset:37888\n\t"
        "ds_read_b128 v[80:83], v117 offset:32768\n\t"
        "ds_read_b128 v[84:87], v117 offset:36864\n\t"
        "s_waitcnt lgkmcnt(5)\n\t"
        "v_mfma_f32_32x32x16_bf16 v[32:47], v[64:67], v[68:71], v[32:47]\n\t"
        "s_waitcnt lgkmcnt(4)\n\t"
        "v_mfma_f32_32x32x16_bf16 v[48:63], v[64:67], v[72:75], v[48:63]\n\t"
        "ds_read_b64_tr_b16 v[88:89], v121 offset:40960\n\t"
        "ds_read_b64_tr_b16 v[90:91], v121 offset:41984\n\t"
        "ds_read_b128 v[92:95], v118 offset:32768\n\t"
        "ds_read_b128 v[96:99], v118 offset:36864\n\t"
        "s_waitcnt lgkmcnt(5)\n\t"
        "v_mfma_f32_32x32x16_bf16 v[32:47], v[76:79], v[80:83], v[32:47]\n\t"
        "s_waitcnt lgkmcnt(4)\n\t"
        "v_mfma_f32_32x32x16_bf16 v[48:63], v[76:79], v[84:87], v[48:63]\n\t"
        "s_waitcnt vmcnt(4)\n\t"
        "s_barrier\n\t"
        "s_add_u32 m0, s26, 0\n\t"
        "s_nop 0\n\t"
        "global_load_lds_dwordx4 v122, s[22:23]\n\t"
        "global_load_lds_dwordx4 v123, s[22:23] offset:1024\n\t"
        "s_add_u32 m0, s26, 16384\n\t"
        "s_nop 0\n\t"
        "global_load_lds_dwordx4 v124, s[24:25]\n\t"
        "global_load_lds_dwordx4 v125, s[24:25] offset:1024\n\t"
        "s_add_u32 s22, s22, 128\n\t"
        "s_addc_u32 s23, s23, 0\n\t"
        "s_add_u32 s24, s24, 221184\n\t"
        "s_addc_u32 s25, s25, 0\n\t"
        "s_cmp_lg_u32 %[wv], 0\n\t"
        "s_cbranch_scc1 2f\n\t"
        "s_mov_b64 s[28:29], exec\n\t"
        "s_mov_b64 exec, 1\n\t"
        "global_store_dwordx2 v191, v[196:197], %[epb] sc1\n\t"
        "s_mov_b64 exec, s[28:29]\n\t"
        "2:\n\t"
        "ds_read_b64_tr_b16 v[100:101], v121 offset:45056\n\t"
        "ds_read_b64_tr_b16 v[102:103], v121 offset:46080\n\t"
        "ds_read_b128 v[104:107], v119 offset:32768\n\t"
        "ds_read_b128 v[108:111], v119 offset:36864\n\t"
        "s_waitcnt lgkmcnt(5)\n\t"
        "v_mfma_f32_32x32x16_bf16 v[32:47], v[88:91], v[92:95], v[32:47]\n\t"
        "s_waitcnt lgkmcnt(4)\n\t"
        "v_mfma_f32_32x32x16_bf16 v[48:63], v[88:91], v[96:99], v[48:63]\n\t"
        "ds_read_b64_tr_b16 v[64:65], v121 offset:0\n\t"
        "ds_read_b64_tr_b16 v[66:67], v121 offset:1024\n\t"
        "ds_read_b128 v[68:71], v116 offset:0\n\t"
        "ds_read_b128 v[72:75], v116 offset:4096\n\t"
        "s_waitcnt lgkmcnt(5)\n\t"
        "v_mfma_f32_32x32x16_bf16 v[32:47], v[100:103], v[104:107], v[32:47]\n\t"
        "s_waitcnt lgkmcnt(4)\n\t"
        "v_mfma_f32_32x32x16_bf16 v[48:63], v[100:103], v[108:111], v[48:63]\n\t"
        "ds_read_b64_tr_b16 v[76:77], v121 offset:4096\n\t"
        "ds_read_b64_tr_b16 v[78:79], v121 offset:5120\n\t"
        "ds_read_b128 v[80:83], v117 offset:0\n\t"
        "ds_read_b128 v[84:87], v117 offset:4096\n\t"
        "s_waitcnt lgkmcnt(5)\n\t"
        "v_mfma_f32_32x32x16_bf16 v[32:47], v[64:67], v[68:71], v[32:47]\n\t"
        "s_waitcnt lgkmcnt(4)\n\t"
        "v_mfma_f32_32x32x16_bf16 v[48:63], v[64:67], v[72:75], v[48:63]\n\t"
        "ds_read_b64_tr_b16 v[88:89], v121 offset:8192\n\t"
        "ds_read_b64_tr_b16 v[90:91], v121 offset:9216\n\t"
        "ds_read_b128 v[92:95], v118 offset:0\n\t"
        "ds_read_b128 v[96:99], v118 offset:4096\n\t"
        "s_waitcnt lgkmcnt(5)\n\t"
        "v_mfma_f32_32x32x16_bf16 v[32:47], v[76:79], v[80:83], v[32:47]\n\t"
        "s_waitcnt lgkmcnt(4)\n\t"
        "v_mfma_f32_32x32x16_bf16 v[48:63], v[76:79], v[84:87], v[48:63]\n\t"
        "s_waitcnt vmcnt(4)\n\t"
        "s_barrier\n\t"
        "s_mov_b32 m0, s48\n\t"
        "s_nop 0\n\t"
        "global_load_lds_dwordx4 %[l16], s[44:45]\n\t"
        "global_load_lds_dwordx4 %[l16], s[44:45] offset:1024\n\t"
        "global_load_lds_dwordx4 %[l16], s[44:45] offset:2048\n\t"
        "s_lshr_b64 exec, exec, 1\n\t"
        "global_load_lds_dwordx4 %[l16], s[44:45] offset:3072\n\t"
        "s_mov_b64 exec, -1\n\t"
        "ds_read_b64_tr_b16 v[100:101], v121 offset:12288\n\t"
        "ds_read_b64_tr_b16 v[102:103], v121 offset:13312\n\t"
        "ds_read_b128 v[104:107], v119 offset:0\n\t"
        "ds_read_b128 v[108:111], v119 offset:4096\n\t"
        "s_waitcnt lgkmcnt(5)\n\t"
        "v_mfma_f32_32x32x16_bf16 v[32:47], v[88:91], v[92:95], v[32:47]\n\t"
        "s_waitcnt lgkmcnt(4)\n\t"
        "v_mfma_f32_32x32x16_bf16 v[48:63], v[88:91], v[96:99], v[48:63]\n\t"
        "ds_read_b64_tr_b16 v[64:65], v120 offset:32768\n\t"
        "ds_read_b64_tr_b16 v[66:67], v120 offset:33792\n\t"
        "ds_read_b128 v[68:71], v112 offset:32768\n\t"
        "ds_read_b128 v[72:75], v112 offset:36864\n\t"
        "s_waitcnt lgkmcnt(5)\n\t"
        "v_mfma_f32_32x32x16_bf16 v[32:47], v[100:103], v[104:107], v[32:47]\n\t"
        "s_waitcnt lgkmcnt(4)\n\t"
        "v_mfma_f32_32x32x16_bf16 v[48:63], v[100:103], v[108:111], v[48:63]\n\t"
        "ds_read_b64_tr_b16 v[76:77], v120 offset:36864\n\t"
        "ds_read_b64_tr_b16 v[78:79], v120 offset:37888\n\t"
        "ds_read_b128 v[80:83], v113 offset:32768\n\t"
        "ds_read_b128 v[84:87], v113 offset:36864\n\t"
        "s_waitcnt lgkmcnt(5)\n\t"
        "v_mfma_f32_32x32x16_bf16 v[32:47], v[64:67], v[68:71], v[32:47]\n\t"
        "s_waitcnt lgkmcnt(4)\n\t"
        "v_mfma_f32_32x32x16_bf16 v[48:63], v[64:67], v[72:75], v[48:63]\n\t"
        "ds_read_b64_tr_b16 v[88:89], v120 offset:40960\n\t"
        "ds_read_b64_tr_b16 v[90:91], v120 offset:41984\n\t"
        "ds_read_b128 v[92:95], v114 offset:32768\n\t"
        "ds_read_b128 v[96:99], v114 offset:36864\n\t"
        "s_waitcnt lgkmcnt(5)\n\t"
        "v_mfma_f32_32x32x16_bf16 v[32:47], v[76:79], v[80:83], v[32:47]\n\t"
        "s_waitcnt lgkmcnt(4)\n\t"
        "v_mfma_f32_32x32x16_bf16 v[48:63], v[76:79], v[84:87], v[48:63]\n\t"
        "s_waitcnt vmcnt(4)\n\t"
        "s_barrier\n\t"
        "s_mov_b32 m0, s49\n\t"
        "s_nop 0\n\t"
        "global_load_lds_dwordx4 %[l16], s[46:47]\n\t"
        "s_mov_b64 exec, s[50:51]\n\t"
        "global_load_lds_dwordx4 %[l16], s[46:47] offset:1024\n\t"
        "s_mov_b64 exec, -1\n\t"
        "ds_read_b64_tr_b16 v[100:101], v120 offset:45056\n\t"
        "ds_read_b64_tr_b16 v[102:103], v120 offset:46080\n\t"
        "ds_read_b128 v[104:107], v115 offset:32768\n\t"
        "ds_read_b128 v[108:111], v115 offset:36864\n\t"
        "s_waitcnt lgkmcnt(5)\n\t"
        "v_mfma_f32_32x32x16_bf16 v[32:47], v[88:91], v[92:95], v[32:47]\n\t"
        "s_waitcnt lgkmcnt(4)\n\t"
        "v_mfma_f32_32x32x16_bf16 v[48:63], v[88:91], v[96:99], v[48:63]\n\t"
        "ds_read_b64_tr_b16 v[64:65], v120 offset:0\n\t"
        "ds_read_b64_tr_b16 v[66:67], v120 offset:1024\n\t"
        "ds_read_b128 v[68:71], v112 offset:0\n\t"
        "ds_read_b128 v[72:75], v112 offset:4096\n\t"
        "s_waitcnt lgkmcnt(5)\n\t"
        "v_mfma_f32_32x32x16_bf16 v[32:47], v[100:103], v[104:107], v[32:47]\n\t"
        "s_waitcnt lgkmcnt(4)\n\t"
        "v_mfma_f32_32x32x16_bf16 v[48:63], v[100:103], v[108:111], v[48:63]\n\t"
        "ds_read_b64_tr_b16 v[76:77], v120 offset:4096\n\t"
        "ds_read_b64_tr_b16 v[78:79], v120 offset:5120\n\t"
        "ds_read_b128 v[80:83], v113 offset:0\n\t"
        "ds_read_b128 v[84:87], v113 offset:4096\n\t"
        "s_waitcnt lgkmcnt(5)\n\t"
        "v_mfma_f32_32x32x16_bf16 v[32:47], v[64:67], v[68:71], v[32:47]\n\t"
        "s_waitcnt lgkmcnt(4)\n\t"
        "v_mfma_f32_32x32x16_bf16 v[48:63], v[64:67], v[72:75], v[48:63]\n\t"
        "ds_read_b64_tr_b16 v[88:89], v120 offset:8192\n\t"
        "ds_read_b64_tr_b16 v[90:91], v120 offset:9216\n\t"
        "ds_read_b128 v[92:95], v114 offset:0\n\t"
        "ds_read_b128 v[96:99], v114 offset:4096\n\t"
        "s_waitcnt lgkmcnt(5)\n\t"
        "v_mfma_f32_32x32x16_bf16 v[32:47], v[76:79], v[80:83], v[32:47]\n\t"
        "s_waitcnt lgkmcnt(4)\n\t"
        "v_mfma_f32_32x32x16_bf16 v[48:63], v[76:79], v[84:87], v[48:63]\n\t"
        "ds_read_b64_tr_b16 v[100:101], v120 offset:12288\n\t"
        "ds_read_b64_tr_b16 v[102:103], v120 offset:13312\n\t"
        "ds_read_b128 v[104:107], v115 offset:0\n\t"
        "ds_read_b128 v[108:111], v115 offset:4096\n\t"
        "s_waitcnt lgkmcnt(5)\n\t"
        "v_mfma_f32_32x32x16_bf16 v[32:47], v[88:91], v[92:95], v[32:47]\n\t"
        "s_waitcnt lgkmcnt(4)\n\t"
        "v_mfma_f32_32x32x16_bf16 v[48:63], v[88:91], v[96:99], v[48:63]\n\t"
        "s_waitcnt lgkmcnt(1)\n\t"
        "v_mfma_f32_32x32x16_bf16 v[32:47], v[100:103], v[104:107], v[32:47]\n\t"
        "s_waitcnt lgkmcnt(0)\n\t"
        "v_mfma_f32_32x32x16_bf16 v[48:63], v[100:103], v[108:111], v[48:63]\n\t"
        "s_mov_b32 m0, s21\n\t"
        "s_nop 15\n\t"
        "s_nop 3\n\t"
        "s_branch 19f\n\t"
        "11:\n\t"
        "s_mov_b32 s21, m0\n\t"
        "s_mov_b64 s[22:23], %[xg]\n\t"
        "s_mov_b64 s[24:25], %[wg]\n\t"
        "s_mov_b32 s26, %[lw]\n\t"
        "s_add_u32 s22, s22, 256\n\t"
        "s_addc_u32 s23, s23, 0\n\t"
        "s_add_u32 s24, s24, 442368\n\t"
        "s_addc_u32 s25, s25, 0\n\t"
        "s_mov_b32 %[uok], 1\n\t"
        "s_mov_b32 s30, 0x05040100\n\t"
        "s_mov_b32 s31, 0x07060302\n\t"
        "v_mov_b32 v172, 0\n\t"
        "global_load_dwordx2 v[196:197], v172, %[epb] sc1\n\t"
        "global_load_dwordx4 v[192:195], %[hdo], %[ssmk]\n\t"
        "global_load_ushort v198, %[dvo], %[ssmd]\n\t"
        "v_add_u32 v116, 0x10000, v112\n\t"
        "v_add_u32 v117, 0x10000, v113\n\t"
        "v_add_u32 v118, 0x10000, v114\n\t"
        "v_add_u32 v119, 0x10000, v115\n\t"
        "v_mov_b32 v120, %[wa]\n\t"
        "v_add_u32 v121, 0x10000, %[wa]\n\t"
        "v_mov_b32 v32, 0\n\t"
        "v_mov_b32 v33, 0\n\t"
        "v_mov_b32 v34, 0\n\t"
        "v_mov_b32 v35, 0\n\t"
        "v_mov_b32 v36, 0\n\t"
        "v_mov_b32 v37, 0\n\t"
        "v_mov_b32 v38, 0\n\t"
        "v_mov_b32 v39, 0\n\t"
        "v_mov_b32 v40, 0\n\t"
        "v_mov_b32 v41, 0\n\t"
        "v_mov_b32 v42, 0\n\t"
        "v_mov_b32 v43, 0\n\t"
        "v_mov_b32 v44, 0\n\t"
        "v_mov_b32 v45, 0\n\t"
        "v_mov_b32 v46, 0\n\t"
        "v_mov_b32 v47, 0\n\t"
        "v_mov_b32 v48, 0\n\t"
        "v_mov_b32 v49, 0\n\t"
        "v_mov_b32 v50, 0\n\t"
        "v_mov_b32 v51, 0\n\t"
        "v_mov_b32 v52, 0\n\t"
        "v_mov_b32 v53, 0\n\t"
        "v_mov_b32 v54, 0\n\t"
        "v_mov_b32 v55, 0\n\t"
        "v_mov_b32 v56, 0\n\t"
        "v_mov_b32 v57, 0\n\t"
        "v_mov_b32 v58, 0\n\t"
        "v_mov_b32 v59, 0\n\t"
        "v_mov_b32 v60, 0\n\t"
        "v_mov_b32 v61, 0\n\t"
        "v_mov_b32 v62, 0\n\t"
        "v_mov_b32 v63, 0\n\t"
        "v_mov_b32 v208, 0\n\t"
        "v_mov_b32 v209, 0\n\t"
        "v_mov_b32 v210, 0\n\t"
        "v_mov_b32 v211, 0\n\t"
        "v_mov_b32 v212, 0\n\t"
        "v_mov_b32 v213, 0\n\t"
        "v_mov_b32 v214, 0\n\t"
        "v_mov_b32 v215, 0\n\t"
        "v_mov_b32 v216, 0\n\t"
        "s_waitcnt vmcnt(7)\n\t"
        "s_barrier\n\t"
        "ds_read_b64_tr_b16 v[64:65], v120 offset:0\n\t"
        "ds_read_b64_tr_b16 v[66:67], v120 offset:1024\n\t"
        "ds_read_b128 v[68:71], v112 offset:0\n\t"
        "ds_read_b128 v[72:75], v112 offset:4096\n\t"
        "ds_read_b64_tr_b16 v[76:77], v120 offset:4096\n\t"
        "ds_read_b64_tr_b16 v[78:79], v120 offset:5120\n\t"
        "ds_read_b128 v[80:83], v113 offset:0\n\t"
        "ds_read_b128 v[84:87], v113 offset:4096\n\t"
        "s_mov_b32 m0, s42\n\t"
        "s_mov_b64 exec, %[pex]\n\t"
        "global_load_lds_dwordx4 v204, s[36:37]\n\t"
        "s_mov_b64 exec, -1\n\t"
        "s_add_u32 m0, s42, 10240\n\t"
        "s_nop 0\n\t"
        "global_load_lds_dwordx4 v205, s[38:39]\n\t"
        "s_mov_b32 m0, s43\n\t"
        "s_nop 0\n\t"
        "global_load_lds_dwordx4 v206, s[40:41]\n\t"
        "s_add_u32 m0, s26, 65536\n\t"
        "s_nop 0\n\t"
        "global_load_lds_dwordx4 v122, s[22:23]\n\t"
        "global_load_lds_dwordx4 v123, s[22:23] offset:1024\n\t"
        "s_add_u32 m0, s26, 81920\n\t"
        "s_nop 0\n\t"
        "global_load_lds_dwordx4 v124, s[24:25]\n\t"
        "global_load_lds_dwordx4 v125, s[24:25] offset:1024\n\t"
        "s_add_u32 s22, s22, 128\n\t"
        "s_addc_u32 s23, s23, 0\n\t"
        "s_add_u32 s24, s24, 221184\n\t"
        "s_addc_u32 s25, s25, 0\n\t"
        "s_waitcnt lgkmcnt(5)\n\t"
        "v_mfma_f32_32x32x16_bf16 v[32:47], v[64:67], v[68:71], v[32:47]\n\t"
        "s_waitcnt lgkmcnt(4)\n\t"
        "v_mfma_f32_32x32x16_bf16 v[48:63], v[64:67], v[72:75], v[48:63]\n\t"
        "ds_read_b64_tr_b16 v[88:89], v120 offset:8192\n\t"
        "ds_read_b64_tr_b16 v[90:91], v120 offset:9216\n\t"
        "ds_read_b128 v[92:95], v114 offset:0\n\t"
        "ds_read_b128 v[96:99], v114 offset:4096\n\t"
        "s_waitcnt lgkmcnt(5)\n\t"
        "v_mfma_f32_32x32x16_bf16 v[32:47], v[76:79], v[80:83], v[32:47]\n\t"
        "s_waitcnt lgkmcnt(4)\n\t"
        "v_mfma_f32_32x32x16_bf16 v[48:63], v[76:79], v[84:87], v[48:63]\n\t"
        "s_waitcnt vmcnt(10)\n\t"
        "s_barrier\n\t"
        "s_add_u32 m0, s26, 32768\n\t"
        "s_nop 0\n\t"
        "global_load_lds_dwordx4 v122, s[22:23]\n\t"
        "global_load_lds_dwordx4 v123, s[22:23] offset:1024\n\t"
        "s_add_u32 m0, s26, 49152\n\t"
        "s_nop 0\n\t"
        "global_load_lds_dwordx4 v124, s[24:25]\n\t"
        "global_load_lds_dwordx4 v125, s[24:25] offset:1024\n\t"
        "s_add_u32 s22, s22, 128\n\t"
        "s_addc_u32 s23, s23, 0\n\t"
        "s_add_u32 s24, s24, 221184\n\t"
        "s_addc_u32 s25, s25, 0\n\t"
        "ds_read_b64_tr_b16 v[100:101], v120 offset:12288\n\t"
        "ds_read_b64_tr_b16 v[102:103], v120 offset:13312\n\t"
        "ds_read_b128 v[104:107], v115 offset:0\n\t"
        "ds_read_b128 v[108:111], v115 offset:4096\n\t"
        "s_waitcnt lgkmcnt(5)\n\t"
        "v_mfma_f32_32x32x16_bf16 v[32:47], v[88:91], v[92:95], v[32:47]\n\t"
        "s_waitcnt lgkmcnt(4)\n\t"
        "v_mfma_f32_32x32x16_bf16 v[48:63], v[88:91], v[96:99], v[48:63]\n\t"
        "ds_read_b64_tr_b16 v[64:65], v121 offset:32768\n\t"
        "ds_read_b64_tr_b16 v[66:67], v121 offset:33792\n\t"
        "ds_read_b128 v[68:71], v116 offset:32768\n\t"
        "ds_read_b128 v[72:75], v116 offset:36864\n\t"
        "s_waitcnt lgkmcnt(5)\n\t"
        "v_mfma_f32_32x32x16_bf16 v[32:47], v[100:103], v[104:107], v[32:47]\n\t"
        "s_waitcnt lgkmcnt(4)\n\t"
        "v_mfma_f32_32x32x16_bf16 v[48:63], v[100:103], v[108:111], v[48:63]\n\t"
        "ds_read_b64_tr_b16 v[76:77], v121 offset:36864\n\t"
        "ds_read_b64_tr_b16 v[78:79], v121 offset:37888\n\t"
        "ds_read_b128 v[80:83], v117 offset:32768\n\t"
        "ds_read_b128 v[84:87], v117 offset:36864\n\t"
        "s_waitcnt lgkmcnt(5)\n\t"
        "v_mfma_f32_32x32x16_bf16 v[32:47], v[64:67], v[68:71], v[32:47]\n\t"
        "s_waitcnt lgkmcnt(4)\n\t"
        "v_mfma_f32_32x32x16_bf16 v[48:63], v[64:67], v[72:75], v[48:63]\n\t"
        "ds_read_b64_tr_b16 v[88:89], v121 offset:40960\n\t"
        "ds_read_b64_tr_b16 v[90:91], v121 offset:41984\n\t"
        "ds_read_b128 v[92:95], v118 offset:32768\n\t"
        "ds_read_b128 v[96:99], v118 offset:36864\n\t"
        "s_waitcnt lgkmcnt(5)\n\t"
        "v_mfma_f32_32x32x16_bf16 v[32:47], v[76:79], v[80:83], v[32:47]\n\t"
        "s_waitcnt lgkmcnt(4)\n\t"
        "v_mfma_f32_32x32x16_bf16 v[48:63], v[76:79], v[84:87], v[48:63]\n\t"
        "s_waitcnt vmcnt(4)\n\t"
        "s_barrier\n\t"
        "s_add_u32 m0, s26, 0\n\t"
        "s_nop 0\n\t"
        "global_load_lds_dwordx4 v122, s[22:23]\n\t"
        "global_load_lds_dwordx4 v123, s[22:23] offset:1024\n\t"
        "s_add_u32 m0, s26, 16384\n\t"
        "s_nop 0\n\t"
        "global_load_lds_dwordx4 v124, s[24:25]\n\t"
        "global_load_lds_dwordx4 v125, s[24:25] offset:1024\n\t"
        "s_add_u32 s22, s22, 128\n\t"
        "s_addc_u32 s23, s23, 0\n\t"
        "s_add_u32 s24, s24, 221184\n\t"
        "s_addc_u32 s25, s25, 0\n\t"
        "ds_read_b64_tr_b16 v[100:101], v121 offset:45056\n\t"
        "ds_read_b64_tr_b16 v[102:103], v121 offset:46080\n\t"
        "ds_read_b128 v[104:107], v119 offset:32768\n\t"
        "ds_read_b128 v[108:111], v119 offset:36864\n\t"
        "s_waitcnt lgkmcnt(5)\n\t"
        "v_mfma_f32_32x32x16_bf16 v[32:47], v[88:91], v[92:95], v[32:47]\n\t"
        "s_waitcnt lgkmcnt(4)\n\t"
        "v_mfma_f32_32x32x16_bf16 v[48:63], v[88:91], v[96:99], v[48:63]\n\t"
        "ds_read_b64_tr_b16 v[64:65], v121 offset:0\n\t"
        "ds_read_b64_tr_b16 v[66:67], v121 offset:1024\n\t"
        "ds_read_b128 v[68:71], v116 offset:0\n\t"
        "ds_read_b128 v[72:75], v116 offset:4096\n\t"
        "ds_read_u16_d16_hi v208, v200 offset:0\n\t"
        "ds_read_u16_d16_hi v209, v200 offset:144\n\t"
        "ds_read_u16_d16_hi v210, v200 offset:288\n\t"
        "ds_read_u16_d16_hi v211, v200 offset:432\n\t"
        "ds_read_u16_d16_hi v212, v200 offset:10240\n\t"
        "ds_read_u16_d16_hi v213, v201 offset:6\n\t"
        "ds_read_u16_d16_hi v214, v201 offset:4\n\t"
        "ds_read_u16_d16_hi v215, v201 offset:2\n\t"
        "ds_read_u16_d16_hi v216, v201 offset:0\n\t"
        "s_waitcnt lgkmcnt(14)\n\t"
        "v_mfma_f32_32x32x16_bf16 v[32:47], v[100:103], v[104:107], v[32:47]\n\t"
        "s_waitcnt lgkmcnt(13)\n\t"
        "v_mfma_f32_32x32x16_bf16 v[48:63], v[100:103], v[108:111], v[48:63]\n\t"
        "ds_read_b64_tr_b16 v[76:77], v121 offset:4096\n\t"
        "ds_read_b64_tr_b16 v[78:79], v121 offset:5120\n\t"
        "ds_read_b128 v[80:83], v117 offset:0\n\t"
        "ds_read_b128 v[84:87], v117 offset:4096\n\t"
        "s_waitcnt lgkmcnt(14)\n\t"
        "v_mfma_f32_32x32x16_bf16 v[32:47], v[64:67], v[68:71], v[32:47]\n\t"
        "s_waitcnt lgkmcnt(13)\n\t"
        "v_mfma_f32_32x32x16_bf16 v[48:63], v[64:67], v[72:75], v[48:63]\n\t"
        "s_waitcnt lgkmcnt(4)\n\t"
        "v_mul_f32 v213, v208, v213\n\t"
        "v_fmac_f32 v213, v209, v214\n\t"
        "v_fmac_f32 v213, v210, v215\n\t"
        "v_fmac_f32 v213, v211, v216\n\t"
        "v_mul_f32 v213, v213, v212\n\t"
        "v_cvt_pk_bf16_f32 v213, v213, v213\n\t"
        "ds_write_b16 v202, v213\n\t"
        "ds_read_b64_tr_b16 v[88:89], v121 offset:8192\n\t"
        "ds_read_b64_tr_b16 v[90:91], v121 offset:9216\n\t"
        "ds_read_b128 v[92:95], v118 offset:0\n\t"
        "ds_read_b128 v[96:99], v118 offset:4096\n\t"
        "s_waitcnt lgkmcnt(6)\n\t"
        "v_mfma_f32_32x32x16_bf16 v[32:47], v[76:79], v[80:83], v[32:47]\n\t"
        "s_waitcnt lgkmcnt(5)\n\t"
        "v_mfma_f32_32x32x16_bf16 v[48:63], v[76:79], v[84:87], v[48:63]\n\t"
        "s_waitcnt vmcnt(4)\n\t"
        "s_barrier\n\t"
        "s_add_u32 m0, s26, 98304\n\t"
        "s_nop 0\n\t"
        "global_load_lds_dwordx4 v122, s[22:23]\n\t"
        "global_load_lds_dwordx4 v123, s[22:23] offset:1024\n\t"
        "s_add_u32 m0, s26, 114688\n\t"
        "s_nop 0\n\t"
        "global_load_lds_dwordx4 v124, s[24:25]\n\t"
        "global_load_lds_dwordx4 v125, s[24:25] offset:1024\n\t"
        "s_add_u32 s22, s22, 128\n\t"
        "s_addc_u32 s23, s23, 0\n\t"
        "s_add_u32 s24, s24, 221184\n\t"
        "s_addc_u32 s25, s25, 0\n\t"
        "ds_read_b64_tr_b16 v[100:101], v121 offset:12288\n\t"
        "ds_read_b64_tr_b16 v[102:103], v121 offset:13312\n\t"
        "ds_read_b128 v[104:107], v119 offset:0\n\t"
        "ds_read_b128 v[108:111], v119 offset:4096\n\t"
        "s_waitcnt lgkmcnt(5)\n\t"
        "v_mfma_f32_32x32x16_bf16 v[32:47], v[88:91], v[92:95], v[32:47]\n\t"
        "s_waitcnt lgkmcnt(4)\n\t"
        "v_mfma_f32_32x32x16_bf16 v[48:63], v[88:91], v[96:99], v[48:63]\n\t"
        "ds_read_b64_tr_b16 v[64:65], v120 offset:32768\n\t"
        "ds_read_b64_tr_b16 v[66:67], v120 offset:33792\n\t"
        "ds_read_b128 v[68:71], v112 offset:32768\n\t"
        "ds_read_b128 v[72:75], v112 offset:36864\n\t"
        "s_waitcnt lgkmcnt(5)\n\t"
        "v_mfma_f32_32x32x16_bf16 v[32:47], v[100:103], v[104:107], v[32:47]\n\t"
        "s_waitcnt lgkmcnt(4)\n\t"
        "v_mfma_f32_32x32x16_bf16 v[48:63], v[100:103], v[108:111], v[48:63]\n\t"
        "ds_read_b64_tr_b16 v[76:77], v120 offset:36864\n\t"
        "ds_read_b64_tr_b16 v[78:79], v120 offset:37888\n\t"
        "ds_read_b128 v[80:83], v113 offset:32768\n\t"
        "ds_read_b128 v[84:87], v113 offset:36864\n\t"
        "s_waitcnt lgkmcnt(5)\n\t"
        "v_mfma_f32_32x32x16_bf16 v[32:47], v[64:67], v[68:71], v[32:47]\n\t"
        "s_waitcnt lgkmcnt(4)\n\t"
        "v_mfma_f32_32x32x16_bf16 v[48:63], v[64:67], v[72:75], v[48:63]\n\t"
        "ds_read_b64_tr_b16 v[88:89], v120 offset:40960\n\t"
        "ds_read_b64_tr_b16 v[90:91], v120 offset:41984\n\t"
        "ds_read_b128 v[92:95], v114 offset:32768\n\t"
        "ds_read_b128 v[96:99], v114 offset:36864\n\t"
        "s_waitcnt lgkmcnt(5)\n\t"
        "v_mfma_f32_32x32x16_bf16 v[32:47], v[76:79], v[80:83], v[32:47]\n\t"
        "s_waitcnt lgkmcnt(4)\n\t"
        "v_mfma_f32_32x32x16_bf16 v[48:63], v[76:79], v[84:87], v[48:63]\n\t"
        "s_waitcnt vmcnt(4)\n\t"
        "s_barrier\n\t"
        "v_add_co_u32 v196, vcc, 1, v196\n\t"
        "s_nop 1\n\t"
        "v_addc_co_u32 v197, vcc, 0, v197, vcc\n\t"
        "s_add_u32 m0, s26, 65536\n\t"
        "s_nop 0\n\t"
        "global_load_lds_dwordx4 v122, s[22:23]\n\t"
        "global_load_lds_dwordx4 v123, s[22:23] offset:1024\n\t"
        "s_add_u32 m0, s26, 81920\n\t"
        "s_nop 0\n\t"
        "global_load_lds_dwordx4 v124, s[24:25]\n\t"
        "global_load_lds_dwordx4 v125, s[24:25] offset:1024\n\t"
        "s_add_u32 s22, s22, 128\n\t"
        "s_addc_u32 s23, s23, 0\n\t"
        "s_add_u32 s24, s24, 221184\n\t"
        "s_addc_u32 s25, s25, 0\n\t"
        "ds_read_b64_tr_b16 v[100:101], v120 offset:45056\n\t"
        "ds_read_b64_tr_b16 v[102:103], v120 offset:46080\n\t"
        "ds_read_b128 v[104:107], v115 offset:32768\n\t"
        "ds_read_b128 v[108:111], v115 offset:36864\n\t"
        "s_waitcnt lgkmcnt(5)\n\t"
        "v_mfma_f32_32x32x16_bf16 v[32:47], v[88:91], v[92:95], v[32:47]\n\t"
        "s_waitcnt lgkmcnt(4)\n\t"
        "v_mfma_f32_32x32x16_bf16 v[48:63], v[88:91], v[96:99], v[48:63]\n\t"
        "ds_read_b64_tr_b16 v[64:65], v120 offset:0\n\t"
        "ds_read_b64_tr_b16 v[66:67], v120 offset:1024\n\t"
        "ds_read_b128 v[68:71], v112 offset:0\n\t"
        "ds_read_b128 v[72:75], v112 offset:4096\n\t"
        "ds_read2_b32 v[128:129], v187 offset0:0 offset1:1\n\t"
        "ds_read2_b32 v[130:131], v187 offset0:2 offset1:3\n\t"
        "ds_read2_b32 v[132:133], v188 offset0:0 offset1:1\n\t"
        "ds_read2_b32 v[134:135], v188 offset0:2 offset1:3\n\t"
        "s_waitcnt lgkmcnt(9)\n\t"
        "v_mfma_f32_32x32x16_bf16 v[32:47], v[100:103], v[104:107], v[32:47]\n\t"
        "s_waitcnt lgkmcnt(8)\n\t"
        "v_mfma_f32_32x32x16_bf16 v[48:63], v[100:103], v[108:111], v[48:63]\n\t"
        "s_waitcnt lgkmcnt(0)\n\t"
        "buffer_store_dwordx4 v[128:131], v189, %[rs], 0 offen sc1\n\t"
        "buffer_store_dwordx4 v[132:135], v190, %[rs], 0 offen sc1\n\t"
        "ds_read_b64_tr_b16 v[76:77], v120 offset:4096\n\t"
        "ds_read_b64_tr_b16 v[78:79], v120 offset:5120\n\t"
        "ds_read_b128 v[80:83], v113 offset:0\n\t"
        "ds_read_b128 v[84:87], v113 offset:4096\n\t"
        "v_mfma_f32_32x32x16_bf16 v[32:47], v[64:67], v[68:71], v[32:47]\n\t"
        "v_mfma_f32_32x32x16_bf16 v[48:63], v[64:67], v[72:75], v[48:63]\n\t"
        "ds_read_b64_tr_b16 v[88:89], v120 offset:8192\n\t"
        "ds_read_b64_tr_b16 v[90:91], v120 offset:9216\n\t"
        "ds_read_b128 v[92:95], v114 offset:0\n\t"
        "ds_read_b128 v[96:99], v114 offset:4096\n\t"
        "s_waitcnt lgkmcnt(5)\n\t"
        "v_mfma_f32_32x32x16_bf16 v[32:47], v[76:79], v[80:83], v[32:47]\n\t"
        "s_waitcnt lgkmcnt(4)\n\t"
        "v_mfma_f32_32x32x16_bf16 v[48:63], v[76:79], v[84:87], v[48:63]\n\t"
        "s_waitcnt vmcnt(6)\n\t"
        "s_barrier\n\t"
        "s_add_u32 m0, s26, 32768\n\t"
        "s_nop 0\n\t"
        "global_load_lds_dwordx4 v122, s[22:23]\n\t"
        "global_load_lds_dwordx4 v123, s[22:23] offset:1024\n\t"
        "s_add_u32 m0, s26, 49152\n\t"
        "s_nop 0\n\t"
        "global_load_lds_dwordx4 v124, s[24:25]\n\t"
        "global_load_lds_dwordx4 v125, s[24:25] offset:1024\n\t"
        "s_add_u32 s22, s22, 128\n\t"
        "s_addc_u32 s23, s23, 0\n\t"
        "s_add_u32 s24, s24, 221184\n\t"
        "s_addc_u32 s25, s25, 0\n\t"
        "ds_read_b64_tr_b16 v[100:101], v120 offset:12288\n\t"
        "ds_read_b64_tr_b16 v[102:103], v120 offset:13312\n\t"
        "ds_read_b128 v[104:107], v115 offset:0\n\t"
        "ds_read_b128 v[108:111], v115 offset:4096\n\t"
        "s_waitcnt lgkmcnt(5)\n\t"
        "v_mfma_f32_32x32x16_bf16 v[32:47], v[88:91], v[92:95], v[32:47]\n\t"
        "s_waitcnt lgkmcnt(4)\n\t"
        "v_mfma_f32_32x32x16_bf16 v[48:63], v[88:91], v[96:99], v[48:63]\n\t"
        "ds_read_b64_tr_b16 v[64:65], v121 offset:32768\n\t"
        "ds_read_b64_tr_b16 v[66:67], v121 offset:33792\n\t"
        "ds_read_b128 v[68:71], v116 offset:32768\n\t"
        "ds_read_b128 v[72:75], v116 offset:36864\n\t"
        "s_waitcnt lgkmcnt(5)\n\t"
        "v_mfma_f32_32x32x16_bf16 v[32:47], v[100:103], v[104:107], v[32:47]\n\t"
        "s_waitcnt lgkmcnt(4)\n\t"
        "v_mfma_f32_32x32x16_bf16 v[48:63], v[100:103], v[108:111], v[48:63]\n\t"
        "ds_read_b64_tr_b16 v[76:77], v121 offset:36864\n\t"
        "ds_read_b64_tr_b16 v[78:79], v121 offset:37888\n\t"
        "ds_read_b128 v[80:83], v117 offset:32768\n\t"
        "ds_read_b128 v[84:87], v117 offset:36864\n\t"
        "s_waitcnt lgkmcnt(5)\n\t"
        "v_mfma_f32_32x32x16_bf16 v[32:47], v[64:67], v[68:71], v[32:47]\n\t"
        "s_waitcnt lgkmcnt(4)\n\t"
        "v_mfma_f32_32x32x16_bf16 v[48:63], v[64:67], v[72:75], v[48:63]\n\t"
        "ds_read_b64_tr_b16 v[88:89], v121 offset:40960\n\t"
        "ds_read_b64_tr_b16 v[90:91], v121 offset:41984\n\t"
        "ds_read_b128 v[92:95], v118 offset:32768\n\t"
        "ds_read_b128 v[96:99], v118 offset:36864\n\t"
        "s_waitcnt lgkmcnt(5)\n\t"
        "v_mfma_f32_32x32x16_bf16 v[32:47], v[76:79], v[80:83], v[32:47]\n\t"
        "s_waitcnt lgkmcnt(4)\n\t"
        "v_mfma_f32_32x32x16_bf16 v[48:63], v[76:79], v[84:87], v[48:63]\n\t"
        "s_waitcnt vmcnt(4)\n\t"
        "s_barrier\n\t"
        "s_add_u32 m0, s26, 0\n\t"
        "s_nop 0\n\t"
        "global_load_lds_dwordx4 v122, s[22:23]\n\t"
        "global_load_lds_dwordx4 v123, s[22:23] offset:1024\n\t"
        "s_add_u32 m0, s26, 16384\n\t"
        "s_nop 0\n\t"
        "global_load_lds_dwordx4 v124, s[24:25]\n\t"
        "global_load_lds_dwordx4 v125, s[24:25] offset:1024\n\t"
        "s_add_u32 s22, s22, 128\n\t"
        "s_addc_u32 s23, s23, 0\n\t"
        "s_add_u32 s24, s24, 221184\n\t"
        "s_addc_u32 s25, s25, 0\n\t"
        "s_cmp_lg_u32 %[wv], 0\n\t"
        "s_cbranch_scc1 2f\n\t"
        "s_mov_b64 s[28:29], exec\n\t"
        "s_mov_b64 exec, 1\n\t"
        "global_store_dwordx2 v191, v[196:197], %[epb] sc1\n\t"
        "s_mov_b64 exec, s[28:29]\n\t"
        "2:\n\t"
        "ds_read_b64_tr_b16 v[100:101], v121 offset:45056\n\t"
        "ds_read_b64_tr_b16 v[102:103], v121 offset:46080\n\t"
        "ds_read_b128 v[104:107], v119 offset:32768\n\t"
        "ds_read_b128 v[108:111], v119 offset:36864\n\t"
        "s_waitcnt lgkmcnt(5)\n\t"
        "v_mfma_f32_32x32x16_bf16 v[32:47], v[88:91], v[92:95], v[32:47]\n\t"
        "s_waitcnt lgkmcnt(4)\n\t"
        "v_mfma_f32_32x32x16_bf16 v[48:63], v[88:91], v[96:99], v[48:63]\n\t"
        "ds_read_b64_tr_b16 v[64:65], v121 offset:0\n\t"
        "ds_read_b64_tr_b16 v[66:67], v121 offset:1024\n\t"
        "ds_read_b128 v[68:71], v116 offset:0\n\t"
        "ds_read_b128 v[72:75], v116 offset:4096\n\t"
        "s_waitcnt lgkmcnt(5)\n\t"
        "v_mfma_f32_32x32x16_bf16 v[32:47], v[100:103], v[104:107], v[32:47]\n\t"
        "s_waitcnt lgkmcnt(4)\n\t"
        "v_mfma_f32_32x32x16_bf16 v[48:63], v[100:103], v[108:111], v[48:63]\n\t"
        "ds_read_b64_tr_b16 v[76:77], v121 offset:4096\n\t"
        "ds_read_b64_tr_b16 v[78:79], v121 offset:5120\n\t"
        "ds_read_b128 v[80:83], v117 offset:0\n\t"
        "ds_read_b128 v[84:87], v117 offset:4096\n\t"
        "s_waitcnt lgkmcnt(5)\n\t"
        "v_mfma_f32_32x32x16_bf16 v[32:47], v[64:67], v[68:71], v[32:47]\n\t"
        "s_waitcnt lgkmcnt(4)\n\t"
        "v_mfma_f32_32x32x16_bf16 v[48:63], v[64:67], v[72:75], v[48:63]\n\t"
        "ds_read_b64_tr_b16 v[88:89], v121 offset:8192\n\t"
        "ds_read_b64_tr_b16 v[90:91], v121 offset:9216\n\t"
        "ds_read_b128 v[92:95], v118 offset:0\n\t"
        "ds_read_b128 v[96:99], v118 offset:4096\n\t"
        "s_waitcnt lgkmcnt(5)\n\t"
        "v_mfma_f32_32x32x16_bf16 v[32:47], v[76:79], v[80:83], v[32:47]\n\t"
        "s_waitcnt lgkmcnt(4)\n\t"
        "v_mfma_f32_32x32x16_bf16 v[48:63], v[76:79], v[84:87], v[48:63]\n\t"
        "s_waitcnt vmcnt(4)\n\t"
        "s_barrier\n\t"
        "s_mov_b32 m0, s48\n\t"
        "s_nop 0\n\t"
        "global_load_lds_dwordx4 %[l16], s[44:45]\n\t"
        "global_load_lds_dwordx4 %[l16], s[44:45] offset:1024\n\t"
        "global_load_lds_dwordx4 %[l16], s[44:45] offset:2048\n\t"
        "s_lshr_b64 exec, exec, 1\n\t"
        "global_load_lds_dwordx4 %[l16], s[44:45] offset:3072\n\t"
        "s_mov_b64 exec, -1\n\t"
        "ds_read_b64_tr_b16 v[100:101], v121 offset:12288\n\t"
        "ds_read_b64_tr_b16 v[102:103], v121 offset:13312\n\t"
        "ds_read_b128 v[104:107], v119 offset:0\n\t"
        "ds_read_b128 v[108:111], v119 offset:4096\n\t"
        "s_waitcnt lgkmcnt(5)\n\t"
        "v_mfma_f32_32x32x16_bf16 v[32:47], v[88:91], v[92:95], v[32:47]\n\t"
        "s_waitcnt lgkmcnt(4)\n\t"
        "v_mfma_f32_32x32x16_bf16 v[48:63], v[88:91], v[96:99], v[48:63]\n\t"
        "ds_read_b64_tr_b16 v[64:65], v120 offset:32768\n\t"
        "ds_read_b64_tr_b16 v[66:67], v120 offset:33792\n\t"
        "ds_read_b128 v[68:71], v112 offset:32768\n\t"
        "ds_read_b128 v[72:75], v112 offset:36864\n\t"
        "s_waitcnt lgkmcnt(5)\n\t"
        "v_mfma_f32_32x32x16_bf16 v[32:47], v[100:103], v[104:107], v[32:47]\n\t"
        "s_waitcnt lgkmcnt(4)\n\t"
        "v_mfma_f32_32x32x16_bf16 v[48:63], v[100:103], v[108:111], v[48:63]\n\t"
        "ds_read_b64_tr_b16 v[76:77], v120 offset:36864\n\t"
        "ds_read_b64_tr_b16 v[78:79], v120 offset:37888\n\t"
        "ds_read_b128 v[80:83], v113 offset:32768\n\t"
        "ds_read_b128 v[84:87], v113 offset:36864\n\t"
        "s_waitcnt lgkmcnt(5)\n\t"
        "v_mfma_f32_32x32x16_bf16 v[32:47], v[64:67], v[68:71], v[32:47]\n\t"
        "s_waitcnt lgkmcnt(4)\n\t"
        "v_mfma_f32_32x32x16_bf16 v[48:63], v[64:67], v[72:75], v[48:63]\n\t"
        "ds_read_b64_tr_b16 v[88:89], v120 offset:40960\n\t"
        "ds_read_b64_tr_b16 v[90:91], v120 offset:41984\n\t"
        "ds_read_b128 v[92:95], v114 offset:32768\n\t"
        "ds_read_b128 v[96:99], v114 offset:36864\n\t"
        "s_waitcnt lgkmcnt(5)\n\t"
        "v_mfma_f32_32x32x16_bf16 v[32:47], v[76:79], v[80:83], v[32:47]\n\t"
        "s_waitcnt lgkmcnt(4)\n\t"
        "v_mfma_f32_32x32x16_bf16 v[48:63], v[76:79], v[84:87], v[48:63]\n\t"
        "s_waitcnt vmcnt(4)\n\t"
        "s_barrier\n\t"
        "s_mov_b32 m0, s49\n\t"
        "s_nop 0\n\t"
        "global_load_lds_dwordx4 %[l16], s[46:47]\n\t"
        "s_mov_b64 exec, s[50:51]\n\t"
        "global_load_lds_dwordx4 %[l16], s[46:47] offset:1024\n\t"
        "s_mov_b64 exec, -1\n\t"
        "ds_read_b64_tr_b16 v[100:101], v120 offset:45056\n\t"
        "ds_read_b64_tr_b16 v[102:103], v120 offset:46080\n\t"
        "ds_read_b128 v[104:107], v115 offset:32768\n\t"
        "ds_read_b128 v[108:111], v115 offset:36864\n\t"
        "s_waitcnt lgkmcnt(5)\n\t"
        "v_mfma_f32_32x32x16_bf16 v[32:47], v[88:91], v[92:95], v[32:47]\n\t"
        "s_waitcnt lgkmcnt(4)\n\t"
        "v_mfma_f32_32x32x16_bf16 v[48:63], v[88:91], v[96:99], v[48:63]\n\t"
        "ds_read_b64_tr_b16 v[64:65], v120 offset:0\n\t"
        "ds_read_b64_tr_b16 v[66:67], v120 offset:1024\n\t"
        "ds_read_b128 v[68:71], v112 offset:0\n\t"
        "ds_read_b128 v[72:75], v112 offset:4096\n\t"
        "s_waitcnt lgkmcnt(5)\n\t"
        "v_mfma_f32_32x32x16_bf16 v[32:47], v[100:103], v[104:107], v[32:47]\n\t"
        "s_waitcnt lgkmcnt(4)\n\t"
        "v_mfma_f32_32x32x16_bf16 v[48:63], v[100:103], v[108:111], v[48:63]\n\t"
        "ds_read_b64_tr_b16 v[76:77], v120 offset:4096\n\t"
        "ds_read_b64_tr_b16 v[78:79], v120 offset:5120\n\t"
        "ds_read_b128 v[80:83], v113 offset:0\n\t"
        "ds_read_b128 v[84:87], v113 offset:4096\n\t"
        "s_waitcnt lgkmcnt(5)\n\t"
        "v_mfma_f32_32x32x16_bf16 v[32:47], v[64:67], v[68:71], v[32:47]\n\t"
        "s_waitcnt lgkmcnt(4)\n\t"
        "v_mfma_f32_32x32x16_bf16 v[48:63], v[64:67], v[72:75], v[48:63]\n\t"
        "ds_read_b64_tr_b16 v[88:89], v120 offset:8192\n\t"
        "ds_read_b64_tr_b16 v[90:91], v120 offset:9216\n\t"
        "ds_read_b128 v[92:95], v114 offset:0\n\t"
        "ds_read_b128 v[96:99], v114 offset:4096\n\t"
        "s_waitcnt lgkmcnt(5)\n\t"
        "v_mfma_f32_32x32x16_bf16 v[32:47], v[76:79], v[80:83], v[32:47]\n\t"
        "s_waitcnt lgkmcnt(4)\n\t"
        "v_mfma_f32_32x32x16_bf16 v[48:63], v[76:79], v[84:87], v[48:63]\n\t"
        "ds_read_b64_tr_b16 v[100:101], v120 offset:12288\n\t"
        "ds_read_b64_tr_b16 v[102:103], v120 offset:13312\n\t"
        "ds_read_b128 v[104:107], v115 offset:0\n\t"
        "ds_read_b128 v[108:111], v115 offset:4096\n\t"
        "s_waitcnt lgkmcnt(5)\n\t"
        "v_mfma_f32_32x32x16_bf16 v[32:47], v[88:91], v[92:95], v[32:47]\n\t"
        "s_waitcnt lgkmcnt(4)\n\t"
        "v_mfma_f32_32x32x16_bf16 v[48:63], v[88:91], v[96:99], v[48:63]\n\t"
        "s_waitcnt lgkmcnt(1)\n\t"
        "v_mfma_f32_32x32x16_bf16 v[32:47], v[100:103], v[104:107], v[32:47]\n\t"
        "s_waitcnt lgkmcnt(0)\n\t"
        "v_mfma_f32_32x32x16_bf16 v[48:63], v[100:103], v[108:111], v[48:63]\n\t"
        "s_mov_b32 m0, s21\n\t"
        "s_nop 15\n\t"
        "s_nop 3\n\t"
        "s_branch 19f\n\t"
        "12:\n\t"
        "s_mov_b32 s21, m0\n\t"
        "s_mov_b64 s[22:23], %[xg]\n\t"
        "s_mov_b64 s[24:25], %[wg]\n\t"
        "s_mov_b32 s26, %[lw]\n\t"
        "s_add_u32 s22, s22, 256\n\t"
        "s_addc_u32 s23, s23, 0\n\t"
        "s_add_u32 s24, s24, 442368\n\t"
        "s_addc_u32 s25, s25, 0\n\t"
        "s_mov_b32 %[uok], 1\n\t"
        "s_mov_b32 s30, 0x05040100\n\t"
        "s_mov_b32 s31, 0x07060302\n\t"
        "v_mov_b32 v172, 0\n\t"
        "global_load_dwordx2 v[196:197], v172, %[epb] sc1\n\t"
        "global_load_dwordx4 v[192:195], %[hdo], %[ssmk]\n\t"
        "global_load_ushort v198, %[dvo], %[ssmd]\n\t"
        "v_add_u32 v116, 0x10000, v112\n\t"
        "v_add_u32 v117, 0x10000, v113\n\t"
        "v_add_u32 v118, 0x10000, v114\n\t"
        "v_add_u32 v119, 0x10000, v115\n\t"
        "v_mov_b32 v120, %[wa]\n\t"
        "v_add_u32 v121, 0x10000, %[wa]\n\t"
        "v_mov_b32 v32, 0\n\t"
        "v_mov_b32 v33, 0\n\t"
        "v_mov_b32 v34, 0\n\t"
        "v_mov_b32 v35, 0\n\t"
        "v_mov_b32 v36, 0\n\t"
        "v_mov_b32 v37, 0\n\t"
        "v_mov_b32 v38, 0\n\t"
        "v_mov_b32 v39, 0\n\t"
        "v_mov_b32 v40, 0\n\t"
        "v_mov_b32 v41, 0\n\t"
        "v_mov_b32 v42, 0\n\t"
        "v_mov_b32 v43, 0\n\t"
        "v_mov_b32 v44, 0\n\t"
        "v_mov_b32 v45, 0\n\t"
        "v_mov_b32 v46, 0\n\t"
        "v_mov_b32 v47, 0\n\t"
        "v_mov_b32 v48, 0\n\t"
        "v_mov_b32 v49, 0\n\t"
        "v_mov_b32 v50, 0\n\t"
        "v_mov_b32 v51, 0\n\t"
        "v_mov_b32 v52, 0\n\t"
        "v_mov_b32 v53, 0\n\t"
        "v_mov_b32 v54, 0\n\t"
        "v_mov_b32 v55, 0\n\t"
        "v_mov_b32 v56, 0\n\t"
        "v_mov_b32 v57, 0\n\t"
        "v_mov_b32 v58, 0\n\t"
        "v_mov_b32 v59, 0\n\t"
        "v_mov_b32 v60, 0\n\t"
        "v_mov_b32 v61, 0\n\t"
        "v_mov_b32 v62, 0\n\t"
        "v_mov_b32 v63, 0\n\t"
        "v_mov_b32 v208, 0\n\t"
        "v_mov_b32 v209, 0\n\t"
        "v_mov_b32 v210, 0\n\t"
        "v_mov_b32 v211, 0\n\t"
        "v_mov_b32 v212, 0\n\t"
        "v_mov_b32 v213, 0\n\t"
        "v_mov_b32 v214, 0\n\t"
        "v_mov_b32 v215, 0\n\t"
        "v_mov_b32 v216, 0\n\t"
        "s_waitcnt vmcnt(7)\n\t"
        "s_barrier\n\t"
        "ds_read_b64_tr_b16 v[64:65], v120 offset:0\n\t"
        "ds_read_b64_tr_b16 v[66:67], v120 offset:1024\n\t"
        "ds_read_b128 v[68:71], v112 offset:0\n\t"
        "ds_read_b128 v[72:75], v112 offset:4096\n\t"
        "ds_read_b64_tr_b16 v[76:77], v120 offset:4096\n\t"
        "ds_read_b64_tr_b16 v[78:79], v120 offset:5120\n\t"
        "ds_read_b128 v[80:83], v113 offset:0\n\t"
        "ds_read_b128 v[84:87], v113 offset:4096\n\t"
        "s_mov_b32 m0, s42\n\t"
        "s_mov_b64 exec, %[pex]\n\t"
        "global_load_lds_dwordx4 v204, s[36:37]\n\t"
        "s_mov_b64 exec, -1\n\t"
        "s_add_u32 m0, s42, 10240\n\t"
        "s_nop 0\n\t"
        "global_load_lds_dwordx4 v205, s[38:39]\n\t"
        "s_mov_b32 m0, s43\n\t"
        "s_nop 0\n\t"
        "global_load_lds_dwordx4 v206, s[40:41]\n\t"
        "s_add_u32 m0, s26, 65536\n\t"
        "s_nop 0\n\t"
        "global_load_lds_dwordx4 v122, s[22:23]\n\t"
        "global_load_lds_dwordx4 v123, s[22:23] offset:1024\n\t"
        "s_add_u32 m0, s26, 81920\n\t"
        "s_nop 0\n\t"
        "global_load_lds_dwordx4 v124, s[24:25]\n\t"
        "global_load_lds_dwordx4 v125, s[24:25] offset:1024\n\t"
        "s_add_u32 s22, s22, 128\n\t"
        "s_addc_u32 s23, s23, 0\n\t"
        "s_add_u32 s24, s24, 221184\n\t"
        "s_addc_u32 s25, s25, 0\n\t"
        "s_waitcnt lgkmcnt(5)\n\t"
        "v_mfma_f32_32x32x16_bf16 v[32:47], v[68:71], v[64:67], v[32:47]\n\t"
        "s_waitcnt lgkmcnt(4)\n\t"
        "v_mfma_f32_32x32x16_bf16 v[48:63], v[72:75], v[64:67], v[48:63]\n\t"
        "ds_read_b64_tr_b16 v[88:89], v120 offset:8192\n\t"
        "ds_read_b64_tr_b16 v[90:91], v120 offset:9216\n\t"
        "ds_read_b128 v[92:95], v114 offset:0\n\t"
        "ds_read_b128 v[96:99], v114 offset:4096\n\t"
        "s_waitcnt lgkmcnt(5)\n\t"
        "v_mfma_f32_32x32x16_bf16 v[32:47], v[80:83], v[76:79], v[32:47]\n\t"
        "s_waitcnt lgkmcnt(4)\n\t"
        "v_mfma_f32_32x32x16_bf16 v[48:63], v[84:87], v[76:79], v[48:63]\n\t"
        "s_waitcnt vmcnt(10)\n\t"
        "s_barrier\n\t"
        "s_add_u32 m0, s26, 32768\n\t"
        "s_nop 0\n\t"
        "global_load_lds_dwordx4 v122, s[22:23]\n\t"
        "global_load_lds_dwordx4 v123, s[22:23] offset:1024\n\t"
        "s_add_u32 m0, s26, 49152\n\t"
        "s_nop 0\n\t"
        "global_load_lds_dwordx4 v124, s[24:25]\n\t"
        "global_load_lds_dwordx4 v125, s[24:25] offset:1024\n\t"
        "s_add_u32 s22, s22, 128\n\t"
        "s_addc_u32 s23, s23, 0\n\t"
        "s_add_u32 s24, s24, 221184\n\t"
        "s_addc_u32 s25, s25, 0\n\t"
        "ds_read_b64_tr_b16 v[100:101], v120 offset:12288\n\t"
        "ds_read_b64_tr_b16 v[102:103], v120 offset:13312\n\t"
        "ds_read_b128 v[104:107], v115 offset:0\n\t"
        "ds_read_b128 v[108:111], v115 offset:4096\n\t"
        "s_waitcnt lgkmcnt(5)\n\t"
        "v_mfma_f32_32x32x16_bf16 v[32:47], v[92:95], v[88:91], v[32:47]\n\t"
        "s_waitcnt lgkmcnt(4)\n\t"
        "v_mfma_f32_32x32x16_bf16 v[48:63], v[96:99], v[88:91], v[48:63]\n\t"
        "ds_read_b64_tr_b16 v[64:65], v121 offset:32768\n\t"
        "ds_read_b64_tr_b16 v[66:67], v121 offset:33792\n\t"
        "ds_read_b128 v[68:71], v116 offset:32768\n\t"
        "ds_read_b128 v[72:75], v116 offset:36864\n\t"
        "s_waitcnt lgkmcnt(5)\n\t"
        "v_mfma_f32_32x32x16_bf16 v[32:47], v[104:107], v[100:103], v[32:47]\n\t"
        "s_waitcnt lgkmcnt(4)\n\t"
        "v_mfma_f32_32x32x16_bf16 v[48:63], v[108:111], v[100:103], v[48:63]\n\t"
        "ds_read_b64_tr_b16 v[76:77], v121 offset:36864\n\t"
        "ds_read_b64_tr_b16 v[78:79], v121 offset:37888\n\t"
        "ds_read_b128 v[80:83], v117 offset:32768\n\t"
        "ds_read_b128 v[84:87], v117 offset:36864\n\t"
        "s_waitcnt lgkmcnt(5)\n\t"
        "v_mfma_f32_32x32x16_bf16 v[32:47], v[68:71], v[64:67], v[32:47]\n\t"
        "s_waitcnt lgkmcnt(4)\n\t"
        "v_mfma_f32_32x32x16_bf16 v[48:63], v[72:75], v[64:67], v[48:63]\n\t"
        "ds_read_b64_tr_b16 v[88:89], v121 offset:40960\n\t"
        "ds_read_b64_tr_b16 v[90:91], v121 offset:41984\n\t"
        "ds_read_b128 v[92:95], v118 offset:32768\n\t"
        "ds_read_b128 v[96:99], v118 offset:36864\n\t"
        "s_waitcnt lgkmcnt(5)\n\t"
        "v_mfma_f32_32x32x16_bf16 v[32:47], v[80:83], v[76:79], v[32:47]\n\t"
        "s_waitcnt lgkmcnt(4)\n\t"
        "v_mfma_f32_32x32x16_bf16 v[48:63], v[84:87], v[76:79], v[48:63]\n\t"
        "s_waitcnt vmcnt(4)\n\t"
        "s_barrier\n\t"
        "s_add_u32 m0, s26, 0\n\t"
        "s_nop 0\n\t"
        "global_load_lds_dwordx4 v122, s[22:23]\n\t"
        "global_load_lds_dwordx4 v123, s[22:23] offset:1024\n\t"
        "s_add_u32 m0, s26, 16384\n\t"
        "s_nop 0\n\t"
        "global_load_lds_dwordx4 v124, s[24:25]\n\t"
        "global_load_lds_dwordx4 v125, s[24:25] offset:1024\n\t"
        "s_add_u32 s22, s22, 128\n\t"
        "s_addc_u32 s23, s23, 0\n\t"
        "s_add_u32 s24, s24, 221184\n\t"
        "s_addc_u32 s25, s25, 0\n\t"
        "ds_read_b64_tr_b16 v[100:101], v121 offset:45056\n\t"
        "ds_read_b64_tr_b16 v[102:103], v121 offset:46080\n\t"
        "ds_read_b128 v[104:107], v119 offset:32768\n\t"
        "ds_read_b128 v[108:111], v119 offset:36864\n\t"
        "s_waitcnt lgkmcnt(5)\n\t"
        "v_mfma_f32_32x32x16_bf16 v[32:47], v[92:95], v[88:91], v[32:47]\n\t"
        "s_waitcnt lgkmcnt(4)\n\t"
        "v_mfma_f32_32x32x16_bf16 v[48:63], v[96:99], v[88:91], v[48:63]\n\t"
        "ds_read_b64_tr_b16 v[64:65], v121 offset:0\n\t"
        "ds_read_b64_tr_b16 v[66:67], v121 offset:1024\n\t"
        "ds_read_b128 v[68:71], v116 offset:0\n\t"
        "ds_read_b128 v[72:75], v116 offset:4096\n\t"
        "ds_read_b128 v[128:131], v184 offset:0\n\t"
        "ds_read_b128 v[132:135], v184 offset:144\n\t"
        "ds_read_b128 v[136:139], v184 offset:288\n\t"
        "ds_read_b128 v[140:143], v184 offset:432\n\t"
        "ds_read_b128 v[144:147], v184 offset:576\n\t"
        "s_waitcnt lgkmcnt(10)\n\t"
        "v_mfma_f32_32x32x16_bf16 v[32:47], v[104:107], v[100:103], v[32:47]\n\t"
        "s_waitcnt lgkmcnt(9)\n\t"
        "v_mfma_f32_32x32x16_bf16 v[48:63], v[108:111], v[100:103], v[48:63]\n\t"
        "ds_read_b64_tr_b16 v[76:77], v121 offset:4096\n\t"
        "ds_read_b64_tr_b16 v[78:79], v121 offset:5120\n\t"
        "ds_read_b128 v[80:83], v117 offset:0\n\t"
        "ds_read_b128 v[84:87], v117 offset:4096\n\t"
        "ds_read_b128 v[148:151], v184 offset:10240\n\t"
        "ds_read_b128 v[152:155], v184 offset:10384\n\t"
        "ds_read_b128 v[156:159], v185 offset:0\n\t"
        "ds_read_b128 v[160:163], v185 offset:16\n\t"
        "ds_read_b128 v[164:167], v185 offset:32\n\t"
        "ds_read_b128 v[168:171], v185 offset:48\n\t"
        "s_waitcnt lgkmcnt(0)\n\t"
        "v_mfma_f32_32x32x16_bf16 v[32:47], v[68:71], v[64:67], v[32:47]\n\t"
        "v_perm_b32 v172, v128, v132, s30\n\t"
        "v_perm_b32 v178, v128, v132, s31\n\t"
        "v_perm_b32 v173, v132, v136, s30\n\t"
        "v_perm_b32 v179, v132, v136, s31\n\t"
        "v_perm_b32 v174, v136, v140, s30\n\t"
        "v_perm_b32 v180, v136, v140, s31\n\t"
        "v_perm_b32 v175, v140, v144, s30\n\t"
        "v_perm_b32 v181, v140, v144, s31\n\t"
        "v_dot2_f32_bf16 v176, v172, v157, 0\n\t"
        "v_dot2_f32_bf16 v182, v178, v159, 0\n\t"
        "v_dot2_f32_bf16 v177, v173, v157, 0\n\t"
        "v_dot2_f32_bf16 v183, v179, v159, 0\n\t"
        "v_dot2_f32_bf16 v176, v174, v156, v176\n\t"
        "v_dot2_f32_bf16 v182, v180, v158, v182\n\t"
        "v_mfma_f32_32x32x16_bf16 v[48:63], v[72:75], v[64:67], v[48:63]\n\t"
        "v_dot2_f32_bf16 v177, v175, v156, v177\n\t"
        "v_dot2_f32_bf16 v183, v181, v158, v183\n\t"
        "v_lshlrev_b32 v172, 16, v148\n\t"
        "v_and_b32 v178, 0xffff0000, v148\n\t"
        "v_lshlrev_b32 v173, 16, v152\n\t"
        "v_and_b32 v179, 0xffff0000, v152\n\t"
        "v_mul_f32 v174, v176, v172\n\t"
        "v_mul_f32 v180, v182, v178\n\t"
        "v_mul_f32 v175, v177, v173\n\t"
        "v_mul_f32 v181, v183, v179\n\t"
        "v_cvt_pk_bf16_f32 v174, v175, v174\n\t"
        "v_cvt_pk_bf16_f32 v180, v181, v180\n\t"
        "ds_write_b32 v186, v174 offset:0\n\t"
        "ds_write_b32 v186, v180 offset:132\n\t"
        "ds_read_b64_tr_b16 v[88:89], v121 offset:8192\n\t"
        "ds_read_b64_tr_b16 v[90:91], v121 offset:9216\n\t"
        "ds_read_b128 v[92:95], v118 offset:0\n\t"
        "ds_read_b128 v[96:99], v118 offset:4096\n\t"
        "v_mfma_f32_32x32x16_bf16 v[32:47], v[80:83], v[76:79], v[32:47]\n\t"
        "v_perm_b32 v172, v129, v133, s30\n\t"
        "v_perm_b32 v178, v129, v133, s31\n\t"
        "v_perm_b32 v173, v133, v137, s30\n\t"
        "v_perm_b32 v179, v133, v137, s31\n\t"
        "v_perm_b32 v174, v137, v141, s30\n\t"
        "v_perm_b32 v180, v137, v141, s31\n\t"
        "v_perm_b32 v175, v141, v145, s30\n\t"
        "v_perm_b32 v181, v141, v145, s31\n\t"
        "v_dot2_f32_bf16 v176, v172, v161, 0\n\t"
        "v_dot2_f32_bf16 v182, v178, v163, 0\n\t"
        "v_dot2_f32_bf16 v177, v173, v161, 0\n\t"
        "v_dot2_f32_bf16 v183, v179, v163, 0\n\t"
        "v_dot2_f32_bf16 v176, v174, v160, v176\n\t"
        "v_dot2_f32_bf16 v182, v180, v162, v182\n\t"
        "v_mfma_f32_32x32x16_bf16 v[48:63], v[84:87], v[76:79], v[48:63]\n\t"
        "v_dot2_f32_bf16 v177, v175, v160, v177\n\t"
        "v_dot2_f32_bf16 v183, v181, v162, v183\n\t"
        "v_lshlrev_b32 v172, 16, v149\n\t"
        "v_and_b32 v178, 0xffff0000, v149\n\t"
        "v_lshlrev_b32 v173, 16, v153\n\t"
        "v_and_b32 v179, 0xffff0000, v153\n\t"
        "v_mul_f32 v174, v176, v172\n\t"
        "v_mul_f32 v180, v182, v178\n\t"
        "v_mul_f32 v175, v177, v173\n\t"
        "v_mul_f32 v181, v183, v179\n\t"
        "v_cvt_pk_bf16_f32 v174, v175, v174\n\t"
        "v_cvt_pk_bf16_f32 v180, v181, v180\n\t"
        "ds_write_b32 v186, v174 offset:264\n\t"
        "ds_write_b32 v186, v180 offset:396\n\t"
        "s_waitcnt vmcnt(4)\n\t"
        "s_barrier\n\t"
        "s_add_u32 m0, s26, 98304\n\t"
        "s_nop 0\n\t"
        "global_load_lds_dwordx4 v122, s[22:23]\n\t"
        "global_load_lds_dwordx4 v123, s[22:23] offset:1024\n\t"
        "s_add_u32 m0, s26, 114688\n\t"
        "s_nop 0\n\t"
        "global_load_lds_dwordx4 v124, s[24:25]\n\t"
        "global_load_lds_dwordx4 v125, s[24:25] offset:1024\n\t"
        "s_add_u32 s22, s22, 128\n\t"
        "s_addc_u32 s23, s23, 0\n\t"
        "s_add_u32 s24, s24, 221184\n\t"
        "s_addc_u32 s25, s25, 0\n\t"
        "ds_read_b64_tr_b16 v[100:101], v121 offset:12288\n\t"
        "ds_read_b64_tr_b16 v[102:103], v121 offset:13312\n\t"
        "ds_read_b128 v[104:107], v119 offset:0\n\t"
        "ds_read_b128 v[108:111], v119 offset:4096\n\t"
        "s_waitcnt lgkmcnt(7)\n\t"
        "v_mfma_f32_32x32x16_bf16 v[32:47], v[92:95], v[88:91], v[32:47]\n\t"
        "v_perm_b32 v172, v130, v134, s30\n\t"
        "v_perm_b32 v178, v130, v134, s31\n\t"
        "v_perm_b32 v173, v134, v138, s30\n\t"
        "v_perm_b32 v179, v134, v138, s31\n\t"
        "v_perm_b32 v174, v138, v142, s30\n\t"
        "v_perm_b32 v180, v138, v142, s31\n\t"
        "v_perm_b32 v175, v142, v146, s30\n\t"
        "v_perm_b32 v181, v142, v146, s31\n\t"
        "v_dot2_f32_bf16 v176, v172, v165, 0\n\t"
        "v_dot2_f32_bf16 v182, v178, v167, 0\n\t"
        "v_dot2_f32_bf16 v177, v173, v165, 0\n\t"
        "v_dot2_f32_bf16 v183, v179, v167, 0\n\t"
        "v_dot2_f32_bf16 v176, v174, v164, v176\n\t"
        "v_dot2_f32_bf16 v182, v180, v166, v182\n\t"
        "s_waitcnt lgkmcnt(6)\n\t"
        "v_mfma_f32_32x32x16_bf16 v[48:63], v[96:99], v[88:91], v[48:63]\n\t"
        "v_dot2_f32_bf16 v177, v175, v164, v177\n\t"
        "v_dot2_f32_bf16 v183, v181, v166, v183\n\t"
        "v_lshlrev_b32 v172, 16, v150\n\t"
        "v_and_b32 v178, 0xffff0000, v150\n\t"
        "v_lshlrev_b32 v173, 16, v154\n\t"
        "v_and_b32 v179, 0xffff0000, v154\n\t"
        "v_mul_f32 v174, v176, v172\n\t"
        "v_mul_f32 v180, v182, v178\n\t"
        "v_mul_f32 v175, v177, v173\n\t"
        "v_mul_f32 v181, v183, v179\n\t"
        "v_cvt_pk_bf16_f32 v174, v175, v174\n\t"
        "v_cvt_pk_bf16_f32 v180, v181, v180\n\t"
        "ds_write_b32 v186, v174 offset:528\n\t"
        "ds_write_b32 v186, v180 offset:660\n\t"
        "ds_read_b64_tr_b16 v[64:65], v120 offset:32768\n\t"
        "ds_read_b64_tr_b16 v[66:67], v120 offset:33792\n\t"
        "ds_read_b128 v[68:71], v112 offset:32768\n\t"
        "ds_read_b128 v[72:75], v112 offset:36864\n\t"
        "s_waitcnt lgkmcnt(7)\n\t"
        "v_mfma_f32_32x32x16_bf16 v[32:47], v[104:107], v[100:103], v[32:47]\n\t"
        "v_perm_b32 v172, v131, v135, s30\n\t"
        "v_perm_b32 v178, v131, v135, s31\n\t"
        "v_perm_b32 v173, v135, v139, s30\n\t"
        "v_perm_b32 v179, v135, v139, s31\n\t"
        "v_perm_b32 v174, v139, v143, s30\n\t"
        "v_perm_b32 v180, v139, v143, s31\n\t"
        "v_perm_b32 v175, v143, v147, s30\n\t"
        "v_perm_b32 v181, v143, v147, s31\n\t"
        "v_dot2_f32_bf16 v176, v172, v169, 0\n\t"
        "v_dot2_f32_bf16 v182, v178, v171, 0\n\t"
        "v_dot2_f32_bf16 v177, v173, v169, 0\n\t"
        "v_dot2_f32_bf16 v183, v179, v171, 0\n\t"
        "v_dot2_f32_bf16 v176, v174, v168, v176\n\t"
        "v_dot2_f32_bf16 v182, v180, v170, v182\n\t"
        "s_waitcnt lgkmcnt(6)\n\t"
        "v_mfma_f32_32x32x16_bf16 v[48:63], v[108:111], v[100:103], v[48:63]\n\t"
        "v_dot2_f32_bf16 v177, v175, v168, v177\n\t"
        "v_dot2_f32_bf16 v183, v181, v170, v183\n\t"
        "v_lshlrev_b32 v172, 16, v151\n\t"
        "v_and_b32 v178, 0xffff0000, v151\n\t"
        "v_lshlrev_b32 v173, 16, v155\n\t"
        "v_and_b32 v179, 0xffff0000, v155\n\t"
        "v_mul_f32 v174, v176, v172\n\t"
        "v_mul_f32 v180, v182, v178\n\t"
        "v_mul_f32 v175, v177, v173\n\t"
        "v_mul_f32 v181, v183, v179\n\t"
        "v_cvt_pk_bf16_f32 v174, v175, v174\n\t"
        "v_cvt_pk_bf16_f32 v180, v181, v180\n\t"
        "ds_write_b32 v186, v174 offset:792\n\t"
        "ds_write_b32 v186, v180 offset:924\n\t"
        "ds_read_b64_tr_b16 v[76:77], v120 offset:36864\n\t"
        "ds_read_b64_tr_b16 v[78:79], v120 offset:37888\n\t"
        "ds_read_b128 v[80:83], v113 offset:32768\n\t"
        "ds_read_b128 v[84:87], v113 offset:36864\n\t"
        "ds_read_u16_d16_hi v208, v200 offset:0\n\t"
        "ds_read_u16_d16_hi v209, v200 offset:144\n\t"
        "ds_read_u16_d16_hi v210, v200 offset:288\n\t"
        "ds_read_u16_d16_hi v211, v200 offset:432\n\t"
        "ds_read_u16_d16_hi v212, v200 offset:10240\n\t"
        "ds_read_u16_d16_hi v213, v201 offset:6\n\t"
        "ds_read_u16_d16_hi v214, v201 offset:4\n\t"
        "ds_read_u16_d16_hi v215, v201 offset:2\n\t"
        "ds_read_u16_d16_hi v216, v201 offset:0\n\t"
        "s_waitcnt lgkmcnt(15)\n\t"
        "v_mfma_f32_32x32x16_bf16 v[32:47], v[68:71], v[64:67], v[32:47]\n\t"
        "s_waitcnt lgkmcnt(15)\n\t"
        "v_mfma_f32_32x32x16_bf16 v[48:63], v[72:75], v[64:67], v[48:63]\n\t"
        "s_waitcnt lgkmcnt(0)\n\t"
        "v_mul_f32 v213, v208, v213\n\t"
        "v_fmac_f32 v213, v209, v214\n\t"
        "v_fmac_f32 v213, v210, v215\n\t"
        "v_fmac_f32 v213, v211, v216\n\t"
        "v_mul_f32 v213, v213, v212\n\t"
        "v_cvt_pk_bf16_f32 v213, v213, v213\n\t"
        "ds_write_b16 v202, v213\n\t"
        "ds_read_b64_tr_b16 v[88:89], v120 offset:40960\n\t"
        "ds_read_b64_tr_b16 v[90:91], v120 offset:41984\n\t"
        "ds_read_b128 v[92:95], v114 offset:32768\n\t"
        "ds_read_b128 v[96:99], v114 offset:36864\n\t"
        "v_mfma_f32_32x32x16_bf16 v[32:47], v[80:83], v[76:79], v[32:47]\n\t"
        "v_mfma_f32_32x32x16_bf16 v[48:63], v[84:87], v[76:79], v[48:63]\n\t"
        "s_waitcnt vmcnt(4) lgkmcnt(4)\n\t"
        "s_barrier\n\t"
        "v_add_co_u32 v196, vcc, 1, v196\n\t"
        "s_nop 1\n\t"
        "v_addc_co_u32 v197, vcc, 0, v197, vcc\n\t"
        "s_add_u32 m0, s26, 65536\n\t"
        "s_nop 0\n\t"
        "global_load_lds_dwordx4 v122, s[22:23]\n\t"
        "global_load_lds_dwordx4 v123, s[22:23] offset:1024\n\t"
        "s_add_u32 m0, s26, 81920\n\t"
        "s_nop 0\n\t"
        "global_load_lds_dwordx4 v124, s[24:25]\n\t"
        "global_load_lds_dwordx4 v125, s[24:25] offset:1024\n\t"
        "s_add_u32 s22, s22, 128\n\t"
        "s_addc_u32 s23, s23, 0\n\t"
        "s_add_u32 s24, s24, 221184\n\t"
        "s_addc_u32 s25, s25, 0\n\t"
        "ds_read_b64_tr_b16 v[100:101], v120 offset:45056\n\t"
        "ds_read_b64_tr_b16 v[102:103], v120 offset:46080\n\t"
        "ds_read_b128 v[104:107], v115 offset:32768\n\t"
        "ds_read_b128 v[108:111], v115 offset:36864\n\t"
        "s_waitcnt lgkmcnt(5)\n\t"
        "v_mfma_f32_32x32x16_bf16 v[32:47], v[92:95], v[88:91], v[32:47]\n\t"
        "s_waitcnt lgkmcnt(4)\n\t"
        "v_mfma_f32_32x32x16_bf16 v[48:63], v[96:99], v[88:91], v[48:63]\n\t"
        "ds_read_b64_tr_b16 v[64:65], v120 offset:0\n\t"
        "ds_read_b64_tr_b16 v[66:67], v120 offset:1024\n\t"
        "ds_read_b128 v[68:71], v112 offset:0\n\t"
        "ds_read_b128 v[72:75], v112 offset:4096\n\t"
        "ds_read2_b32 v[128:129], v187 offset0:0 offset1:1\n\t"
        "ds_read2_b32 v[130:131], v187 offset0:2 offset1:3\n\t"
        "ds_read2_b32 v[132:133], v188 offset0:0 offset1:1\n\t"
        "ds_read2_b32 v[134:135], v188 offset0:2 offset1:3\n\t"
        "s_waitcnt lgkmcnt(9)\n\t"
        "v_mfma_f32_32x32x16_bf16 v[32:47], v[104:107], v[100:103], v[32:47]\n\t"
        "s_waitcnt lgkmcnt(8)\n\t"
        "v_mfma_f32_32x32x16_bf16 v[48:63], v[108:111], v[100:103], v[48:63]\n\t"
        "s_waitcnt lgkmcnt(0)\n\t"
        "buffer_store_dwordx4 v[128:131], v189, %[rs], 0 offen sc1\n\t"
        "buffer_store_dwordx4 v[132:135], v190, %[rs], 0 offen sc1\n\t"
        "ds_read_b64_tr_b16 v[76:77], v120 offset:4096\n\t"
        "ds_read_b64_tr_b16 v[78:79], v120 offset:5120\n\t"
        "ds_read_b128 v[80:83], v113 offset:0\n\t"
        "ds_read_b128 v[84:87], v113 offset:4096\n\t"
        "v_mfma_f32_32x32x16_bf16 v[32:47], v[68:71], v[64:67], v[32:47]\n\t"
        "v_mfma_f32_32x32x16_bf16 v[48:63], v[72:75], v[64:67], v[48:63]\n\t"
        "ds_read_b64_tr_b16 v[88:89], v120 offset:8192\n\t"
        "ds_read_b64_tr_b16 v[90:91], v120 offset:9216\n\t"
        "ds_read_b128 v[92:95], v114 offset:0\n\t"
        "ds_read_b128 v[96:99], v114 offset:4096\n\t"
        "s_waitcnt lgkmcnt(5)\n\t"
        "v_mfma_f32_32x32x16_bf16 v[32:47], v[80:83], v[76:79], v[32:47]\n\t"
        "s_waitcnt lgkmcnt(4)\n\t"
        "v_mfma_f32_32x32x16_bf16 v[48:63], v[84:87], v[76:79], v[48:63]\n\t"
        "s_waitcnt vmcnt(6)\n\t"
        "s_barrier\n\t"
        "s_add_u32 m0, s26, 32768\n\t"
        "s_nop 0\n\t"
        "global_load_lds_dwordx4 v122, s[22:23]\n\t"
        "global_load_lds_dwordx4 v123, s[22:23] offset:1024\n\t"
        "s_add_u32 m0, s26, 49152\n\t"
        "s_nop 0\n\t"
        "global_load_lds_dwordx4 v124, s[24:25]\n\t"
        "global_load_lds_dwordx4 v125, s[24:25] offset:1024\n\t"
        "s_add_u32 s22, s22, 128\n\t"
        "s_addc_u32 s23, s23, 0\n\t"
        "s_add_u32 s24, s24, 221184\n\t"
        "s_addc_u32 s25, s25, 0\n\t"
        "ds_read_b64_tr_b16 v[100:101], v120 offset:12288\n\t"
        "ds_read_b64_tr_b16 v[102:103], v120 offset:13312\n\t"
        "ds_read_b128 v[104:107], v115 offset:0\n\t"
        "ds_read_b128 v[108:111], v115 offset:4096\n\t"
        "s_waitcnt lgkmcnt(5)\n\t"
        "v_mfma_f32_32x32x16_bf16 v[32:47], v[92:95], v[88:91], v[32:47]\n\t"
        "s_waitcnt lgkmcnt(4)\n\t"
        "v_mfma_f32_32x32x16_bf16 v[48:63], v[96:99], v[88:91], v[48:63]\n\t"
        "ds_read_b64_tr_b16 v[64:65], v121 offset:32768\n\t"
        "ds_read_b64_tr_b16 v[66:67], v121 offset:33792\n\t"
        "ds_read_b128 v[68:71], v116 offset:32768\n\t"
        "ds_read_b128 v[72:75], v116 offset:36864\n\t"
        "s_waitcnt lgkmcnt(5)\n\t"
        "v_mfma_f32_32x32x16_bf16 v[32:47], v[104:107], v[100:103], v[32:47]\n\t"
        "s_waitcnt lgkmcnt(4)\n\t"
        "v_mfma_f32_32x32x16_bf16 v[48:63], v[108:111], v[100:103], v[48:63]\n\t"
        "ds_read_b64_tr_b16 v[76:77], v121 offset:36864\n\t"
        "ds_read_b64_tr_b16 v[78:79], v121 offset:37888\n\t"
        "ds_read_b128 v[80:83], v117 offset:32768\n\t"
        "ds_read_b128 v[84:87], v117 offset:36864\n\t"
        "s_waitcnt lgkmcnt(5)\n\t"
        "v_mfma_f32_32x32x16_bf16 v[32:47], v[68:71], v[64:67], v[32:47]\n\t"
        "s_waitcnt lgkmcnt(4)\n\t"
        "v_mfma_f32_32x32x16_bf16 v[48:63], v[72:75], v[64:67], v[48:63]\n\t"
        "ds_read_b64_tr_b16 v[88:89], v121 offset:40960\n\t"
        "ds_read_b64_tr_b16 v[90:91], v121 offset:41984\n\t"
        "ds_read_b128 v[92:95], v118 offset:32768\n\t"
        "ds_read_b128 v[96:99], v118 offset:36864\n\t"
        "s_waitcnt lgkmcnt(5)\n\t"
        "v_mfma_f32_32x32x16_bf16 v[32:47], v[80:83], v[76:79], v[32:47]\n\t"
        "s_waitcnt lgkmcnt(4)\n\t"
        "v_mfma_f32_32x32x16_bf16 v[48:63], v[84:87], v[76:79], v[48:63]\n\t"
        "s_waitcnt vmcnt(4)\n\t"
        "s_barrier\n\t"
        "s_add_u32 m0, s26, 0\n\t"
        "s_nop 0\n\t"
        "global_load_lds_dwordx4 v122, s[22:23]\n\t"
        "global_load_lds_dwordx4 v123, s[22:23] offset:1024\n\t"
        "s_add_u32 m0, s26, 16384\n\t"
        "s_nop 0\n\t"
        "global_load_lds_dwordx4 v124, s[24:25]\n\t"
        "global_load_lds_dwordx4 v125, s[24:25] offset:1024\n\t"
        "s_add_u32 s22, s22, 128\n\t"
        "s_addc_u32 s23, s23, 0\n\t"
        "s_add_u32 s24, s24, 221184\n\t"
        "s_addc_u32 s25, s25, 0\n\t"
        "s_cmp_lg_u32 %[wv], 0\n\t"
        "s_cbranch_scc1 2f\n\t"
        "s_mov_b64 s[28:29], exec\n\t"
        "s_mov_b64 exec, 1\n\t"
        "global_store_dwordx2 v191, v[196:197], %[epb] sc1\n\t"
        "s_mov_b64 exec, s[28:29]\n\t"
        "2:\n\t"
        "ds_read_b64_tr_b16 v[100:101], v121 offset:45056\n\t"
        "ds_read_b64_tr_b16 v[102:103], v121 offset:46080\n\t"
        "ds_read_b128 v[104:107], v119 offset:32768\n\t"
        "ds_read_b128 v[108:111], v119 offset:36864\n\t"
        "s_waitcnt lgkmcnt(5)\n\t"
        "v_mfma_f32_32x32x16_bf16 v[32:47], v[92:95], v[88:91], v[32:47]\n\t"
        "s_waitcnt lgkmcnt(4)\n\t"
        "v_mfma_f32_32x32x16_bf16 v[48:63], v[96:99], v[88:91], v[48:63]\n\t"
        "ds_read_b64_tr_b16 v[64:65], v121 offset:0\n\t"
        "ds_read_b64_tr_b16 v[66:67], v121 offset:1024\n\t"
        "ds_read_b128 v[68:71], v116 offset:0\n\t"
        "ds_read_b128 v[72:75], v116 offset:4096\n\t"
        "s_waitcnt lgkmcnt(5)\n\t"
        "v_mfma_f32_32x32x16_bf16 v[32:47], v[104:107], v[100:103], v[32:47]\n\t"
        "s_waitcnt lgkmcnt(4)\n\t"
        "v_mfma_f32_32x32x16_bf16 v[48:63], v[108:111], v[100:103], v[48:63]\n\t"
        "ds_read_b64_tr_b16 v[76:77], v121 offset:4096\n\t"
        "ds_read_b64_tr_b16 v[78:79], v121 offset:5120\n\t"
        "ds_read_b128 v[80:83], v117 offset:0\n\t"
        "ds_read_b128 v[84:87], v117 offset:4096\n\t"
        "s_waitcnt lgkmcnt(5)\n\t"
        "v_mfma_f32_32x32x16_bf16 v[32:47], v[68:71], v[64:67], v[32:47]\n\t"
        "s_waitcnt lgkmcnt(4)\n\t"
        "v_mfma_f32_32x32x16_bf16 v[48:63], v[72:75], v[64:67], v[48:63]\n\t"
        "ds_read_b64_tr_b16 v[88:89], v121 offset:8192\n\t"
        "ds_read_b64_tr_b16 v[90:91], v121 offset:9216\n\t"
        "ds_read_b128 v[92:95], v118 offset:0\n\t"
        "ds_read_b128 v[96:99], v118 offset:4096\n\t"
        "s_waitcnt lgkmcnt(5)\n\t"
        "v_mfma_f32_32x32x16_bf16 v[32:47], v[80:83], v[76:79], v[32:47]\n\t"
        "s_waitcnt lgkmcnt(4)\n\t"
        "v_mfma_f32_32x32x16_bf16 v[48:63], v[84:87], v[76:79], v[48:63]\n\t"
        "s_waitcnt vmcnt(4)\n\t"
        "s_barrier\n\t"
        "s_mov_b32 m0, s48\n\t"
        "s_nop 0\n\t"
        "global_load_lds_dwordx4 %[l16], s[44:45]\n\t"
        "global_load_lds_dwordx4 %[l16], s[44:45] offset:1024\n\t"
        "global_load_lds_dwordx4 %[l16], s[44:45] offset:2048\n\t"
        "s_lshr_b64 exec, exec, 1\n\t"
        "global_load_lds_dwordx4 %[l16], s[44:45] offset:3072\n\t"
        "s_mov_b64 exec, -1\n\t"
        "ds_read_b64_tr_b16 v[100:101], v121 offset:12288\n\t"
        "ds_read_b64_tr_b16 v[102:103], v121 offset:13312\n\t"
        "ds_read_b128 v[104:107], v119 offset:0\n\t"
        "ds_read_b128 v[108:111], v119 offset:4096\n\t"
        "s_waitcnt lgkmcnt(5)\n\t"
        "v_mfma_f32_32x32x16_bf16 v[32:47], v[92:95], v[88:91], v[32:47]\n\t"
        "s_waitcnt lgkmcnt(4)\n\t"
        "v_mfma_f32_32x32x16_bf16 v[48:63], v[96:99], v[88:91], v[48:63]\n\t"
        "ds_read_b64_tr_b16 v[64:65], v120 offset:32768\n\t"
        "ds_read_b64_tr_b16 v[66:67], v120 offset:33792\n\t"
        "ds_read_b128 v[68:71], v112 offset:32768\n\t"
        "ds_read_b128 v[72:75], v112 offset:36864\n\t"
        "s_waitcnt lgkmcnt(5)\n\t"
        "v_mfma_f32_32x32x16_bf16 v[32:47], v[104:107], v[100:103], v[32:47]\n\t"
        "s_waitcnt lgkmcnt(4)\n\t"
        "v_mfma_f32_32x32x16_bf16 v[48:63], v[108:111], v[100:103], v[48:63]\n\t"
        "ds_read_b64_tr_b16 v[76:77], v120 offset:36864\n\t"
        "ds_read_b64_tr_b16 v[78:79], v120 offset:37888\n\t"
        "ds_read_b128 v[80:83], v113 offset:32768\n\t"
        "ds_read_b128 v[84:87], v113 offset:36864\n\t"
        "s_waitcnt lgkmcnt(5)\n\t"
        "v_mfma_f32_32x32x16_bf16 v[32:47], v[68:71], v[64:67], v[32:47]\n\t"
        "s_waitcnt lgkmcnt(4)\n\t"
        "v_mfma_f32_32x32x16_bf16 v[48:63], v[72:75], v[64:67], v[48:63]\n\t"
        "ds_read_b64_tr_b16 v[88:89], v120 offset:40960\n\t"
        "ds_read_b64_tr_b16 v[90:91], v120 offset:41984\n\t"
        "ds_read_b128 v[92:95], v114 offset:32768\n\t"
        "ds_read_b128 v[96:99], v114 offset:36864\n\t"
        "s_waitcnt lgkmcnt(5)\n\t"
        "v_mfma_f32_32x32x16_bf16 v[32:47], v[80:83], v[76:79], v[32:47]\n\t"
        "s_waitcnt lgkmcnt(4)\n\t"
        "v_mfma_f32_32x32x16_bf16 v[48:63], v[84:87], v[76:79], v[48:63]\n\t"
        "s_waitcnt vmcnt(4)\n\t"
        "s_barrier\n\t"
        "s_mov_b32 m0, s49\n\t"
        "s_nop 0\n\t"
        "global_load_lds_dwordx4 %[l16], s[46:47]\n\t"
        "s_mov_b64 exec, s[50:51]\n\t"
        "global_load_lds_dwordx4 %[l16], s[46:47] offset:1024\n\t"
        "s_mov_b64 exec, -1\n\t"
        "ds_read_b64_tr_b16 v[100:101], v120 offset:45056\n\t"
        "ds_read_b64_tr_b16 v[102:103], v120 offset:46080\n\t"
        "ds_read_b128 v[104:107], v115 offset:32768\n\t"
        "ds_read_b128 v[108:111], v115 offset:36864\n\t"
        "s_waitcnt lgkmcnt(5)\n\t"
        "v_mfma_f32_32x32x16_bf16 v[32:47], v[92:95], v[88:91], v[32:47]\n\t"
        "s_waitcnt lgkmcnt(4)\n\t"
        "v_mfma_f32_32x32x16_bf16 v[48:63], v[96:99], v[88:91], v[48:63]\n\t"
        "ds_read_b64_tr_b16 v[64:65], v120 offset:0\n\t"
        "ds_read_b64_tr_b16 v[66:67], v120 offset:1024\n\t"
        "ds_read_b128 v[68:71], v112 offset:0\n\t"
        "ds_read_b128 v[72:75], v112 offset:4096\n\t"
        "s_waitcnt lgkmcnt(5)\n\t"
        "v_mfma_f32_32x32x16_bf16 v[32:47], v[104:107], v[100:103], v[32:47]\n\t"
        "s_waitcnt lgkmcnt(4)\n\t"
        "v_mfma_f32_32x32x16_bf16 v[48:63], v[108:111], v[100:103], v[48:63]\n\t"
        "ds_read_b64_tr_b16 v[76:77], v120 offset:4096\n\t"
        "ds_read_b64_tr_b16 v[78:79], v120 offset:5120\n\t"
        "ds_read_b128 v[80:83], v113 offset:0\n\t"
        "ds_read_b128 v[84:87], v113 offset:4096\n\t"
        "s_waitcnt lgkmcnt(5)\n\t"
        "v_mfma_f32_32x32x16_bf16 v[32:47], v[68:71], v[64:67], v[32:47]\n\t"
        "s_waitcnt lgkmcnt(4)\n\t"
        "v_mfma_f32_32x32x16_bf16 v[48:63], v[72:75], v[64:67], v[48:63]\n\t"
        "ds_read_b64_tr_b16 v[88:89], v120 offset:8192\n\t"
        "ds_read_b64_tr_b16 v[90:91], v120 offset:9216\n\t"
        "ds_read_b128 v[92:95], v114 offset:0\n\t"
        "ds_read_b128 v[96:99], v114 offset:4096\n\t"
        "s_waitcnt lgkmcnt(5)\n\t"
        "v_mfma_f32_32x32x16_bf16 v[32:47], v[80:83], v[76:79], v[32:47]\n\t"
        "s_waitcnt lgkmcnt(4)\n\t"
        "v_mfma_f32_32x32x16_bf16 v[48:63], v[84:87], v[76:79], v[48:63]\n\t"
        "ds_read_b64_tr_b16 v[100:101], v120 offset:12288\n\t"
        "ds_read_b64_tr_b16 v[102:103], v120 offset:13312\n\t"
        "ds_read_b128 v[104:107], v115 offset:0\n\t"
        "ds_read_b128 v[108:111], v115 offset:4096\n\t"
        "s_waitcnt lgkmcnt(5)\n\t"
        "v_mfma_f32_32x32x16_bf16 v[32:47], v[92:95], v[88:91], v[32:47]\n\t"
        "s_waitcnt lgkmcnt(4)\n\t"
        "v_mfma_f32_32x32x16_bf16 v[48:63], v[96:99], v[88:91], v[48:63]\n\t"
        "s_waitcnt lgkmcnt(1)\n\t"
        "v_mfma_f32_32x32x16_bf16 v[32:47], v[104:107], v[100:103], v[32:47]\n\t"
        "s_waitcnt lgkmcnt(0)\n\t"
        "v_mfma_f32_32x32x16_bf16 v[48:63], v[108:111], v[100:103], v[48:63]\n\t"
        "s_mov_b32 m0, s21\n\t"
        "s_nop 15\n\t"
        "s_nop 3\n\t"
        "s_branch 19f\n\t"
        "13:\n\t"
        "s_mov_b32 s21, m0\n\t"
        "s_mov_b64 s[22:23], %[xg]\n\t"
        "s_mov_b64 s[24:25], %[wg]\n\t"
        "s_mov_b32 s26, %[lw]\n\t"
        "s_add_u32 s22, s22, 256\n\t"
        "s_addc_u32 s23, s23, 0\n\t"
        "s_add_u32 s24, s24, 442368\n\t"
        "s_addc_u32 s25, s25, 0\n\t"
        "s_mov_b32 %[uok], 1\n\t"
        "s_mov_b32 s30, 0x05040100\n\t"
        "s_mov_b32 s31, 0x07060302\n\t"
        "v_mov_b32 v172, 0\n\t"
        "global_load_dwordx2 v[196:197], v172, %[epb] sc1\n\t"
        "global_load_dwordx4 v[192:195], %[hdo], %[ssmk]\n\t"
        "global_load_ushort v198, %[dvo], %[ssmd]\n\t"
        "v_add_u32 v116, 0x10000, v112\n\t"
        "v_add_u32 v117, 0x10000, v113\n\t"
        "v_add_u32 v118, 0x10000, v114\n\t"
        "v_add_u32 v119, 0x10000, v115\n\t"
        "v_mov_b32 v120, %[wa]\n\t"
        "v_add_u32 v121, 0x10000, %[wa]\n\t"
        "v_mov_b32 v32, 0\n\t"
        "v_mov_b32 v33, 0\n\t"
        "v_mov_b32 v34, 0\n\t"
        "v_mov_b32 v35, 0\n\t"
        "v_mov_b32 v36, 0\n\t"
        "v_mov_b32 v37, 0\n\t"
        "v_mov_b32 v38, 0\n\t"
        "v_mov_b32 v39, 0\n\t"
        "v_mov_b32 v40, 0\n\t"
        "v_mov_b32 v41, 0\n\t"
        "v_mov_b32 v42, 0\n\t"
        "v_mov_b32 v43, 0\n\t"
        "v_mov_b32 v44, 0\n\t"
        "v_mov_b32 v45, 0\n\t"
        "v_mov_b32 v46, 0\n\t"
        "v_mov_b32 v47, 0\n\t"
        "v_mov_b32 v48, 0\n\t"
        "v_mov_b32 v49, 0\n\t"
        "v_mov_b32 v50, 0\n\t"
        "v_mov_b32 v51, 0\n\t"
        "v_mov_b32 v52, 0\n\t"
        "v_mov_b32 v53, 0\n\t"
        "v_mov_b32 v54, 0\n\t"
        "v_mov_b32 v55, 0\n\t"
        "v_mov_b32 v56, 0\n\t"
        "v_mov_b32 v57, 0\n\t"
        "v_mov_b32 v58, 0\n\t"
        "v_mov_b32 v59, 0\n\t"
        "v_mov_b32 v60, 0\n\t"
        "v_mov_b32 v61, 0\n\t"
        "v_mov_b32 v62, 0\n\t"
        "v_mov_b32 v63, 0\n\t"
        "v_mov_b32 v208, 0\n\t"
        "v_mov_b32 v209, 0\n\t"
        "v_mov_b32 v210, 0\n\t"
        "v_mov_b32 v211, 0\n\t"
        "v_mov_b32 v212, 0\n\t"
        "v_mov_b32 v213, 0\n\t"
        "v_mov_b32 v214, 0\n\t"
        "v_mov_b32 v215, 0\n\t"
        "v_mov_b32 v216, 0\n\t"
        "s_waitcnt vmcnt(7)\n\t"
        "s_barrier\n\t"
        "ds_read_b64_tr_b16 v[64:65], v120 offset:0\n\t"
        "ds_read_b64_tr_b16 v[66:67], v120 offset:1024\n\t"
        "ds_read_b128 v[68:71], v112 offset:0\n\t"
        "ds_read_b128 v[72:75], v112 offset:4096\n\t"
        "ds_read_b64_tr_b16 v[76:77], v120 offset:4096\n\t"
        "ds_read_b64_tr_b16 v[78:79], v120 offset:5120\n\t"
        "ds_read_b128 v[80:83], v113 offset:0\n\t"
        "ds_read_b128 v[84:87], v113 offset:4096\n\t"
        "s_mov_b32 m0, s42\n\t"
        "s_mov_b64 exec, %[pex]\n\t"
        "global_load_lds_dwordx4 v204, s[36:37]\n\t"
        "s_mov_b64 exec, -1\n\t"
        "s_add_u32 m0, s42, 10240\n\t"
        "s_nop 0\n\t"
        "global_load_lds_dwordx4 v205, s[38:39]\n\t"
        "s_mov_b32 m0, s43\n\t"
        "s_nop 0\n\t"
        "global_load_lds_dwordx4 v206, s[40:41]\n\t"
        "s_add_u32 m0, s26, 65536\n\t"
        "s_nop 0\n\t"
        "global_load_lds_dwordx4 v122, s[22:23]\n\t"
        "global_load_lds_dwordx4 v123, s[22:23] offset:1024\n\t"
        "s_add_u32 m0, s26, 81920\n\t"
        "s_nop 0\n\t"
        "global_load_lds_dwordx4 v124, s[24:25]\n\t"
        "global_load_lds_dwordx4 v125, s[24:25] offset:1024\n\t"
        "s_add_u32 s22, s22, 128\n\t"
        "s_addc_u32 s23, s23, 0\n\t"
        "s_add_u32 s24, s24, 221184\n\t"
        "s_addc_u32 s25, s25, 0\n\t"
        "s_waitcnt lgkmcnt(5)\n\t"
        "v_mfma_f32_32x32x16_bf16 v[32:47], v[68:71], v[64:67], v[32:47]\n\t"
        "s_waitcnt lgkmcnt(4)\n\t"
        "v_mfma_f32_32x32x16_bf16 v[48:63], v[72:75], v[64:67], v[48:63]\n\t"
        "ds_read_b64_tr_b16 v[88:89], v120 offset:8192\n\t"
        "ds_read_b64_tr_b16 v[90:91], v120 offset:9216\n\t"
        "ds_read_b128 v[92:95], v114 offset:0\n\t"
        "ds_read_b128 v[96:99], v114 offset:4096\n\t"
        "s_waitcnt lgkmcnt(5)\n\t"
        "v_mfma_f32_32x32x16_bf16 v[32:47], v[80:83], v[76:79], v[32:47]\n\t"
        "s_waitcnt lgkmcnt(4)\n\t"
        "v_mfma_f32_32x32x16_bf16 v[48:63], v[84:87], v[76:79], v[48:63]\n\t"
        "s_waitcnt vmcnt(10)\n\t"
        "s_barrier\n\t"
        "s_add_u32 m0, s26, 32768\n\t"
        "s_nop 0\n\t"
        "global_load_lds_dwordx4 v122, s[22:23]\n\t"
        "global_load_lds_dwordx4 v123, s[22:23] offset:1024\n\t"
        "s_add_u32 m0, s26, 49152\n\t"
        "s_nop 0\n\t"
        "global_load_lds_dwordx4 v124, s[24:25]\n\t"
        "global_load_lds_dwordx4 v125, s[24:25] offset:1024\n\t"
        "s_add_u32 s22, s22, 128\n\t"
        "s_addc_u32 s23, s23, 0\n\t"
        "s_add_u32 s24, s24, 221184\n\t"
        "s_addc_u32 s25, s25, 0\n\t"
        "ds_read_b64_tr_b16 v[100:101], v120 offset:12288\n\t"
        "ds_read_b64_tr_b16 v[102:103], v120 offset:13312\n\t"
        "ds_read_b128 v[104:107], v115 offset:0\n\t"
        "ds_read_b128 v[108:111], v115 offset:4096\n\t"
        "s_waitcnt lgkmcnt(5)\n\t"
        "v_mfma_f32_32x32x16_bf16 v[32:47], v[92:95], v[88:91], v[32:47]\n\t"
        "s_waitcnt lgkmcnt(4)\n\t"
        "v_mfma_f32_32x32x16_bf16 v[48:63], v[96:99], v[88:91], v[48:63]\n\t"
        "ds_read_b64_tr_b16 v[64:65], v121 offset:32768\n\t"
        "ds_read_b64_tr_b16 v[66:67], v121 offset:33792\n\t"
        "ds_read_b128 v[68:71], v116 offset:32768\n\t"
        "ds_read_b128 v[72:75], v116 offset:36864\n\t"
        "s_waitcnt lgkmcnt(5)\n\t"
        "v_mfma_f32_32x32x16_bf16 v[32:47], v[104:107], v[100:103], v[32:47]\n\t"
        "s_waitcnt lgkmcnt(4)\n\t"
        "v_mfma_f32_32x32x16_bf16 v[48:63], v[108:111], v[100:103], v[48:63]\n\t"
        "ds_read_b64_tr_b16 v[76:77], v121 offset:36864\n\t"
        "ds_read_b64_tr_b16 v[78:79], v121 offset:37888\n\t"
        "ds_read_b128 v[80:83], v117 offset:32768\n\t"
        "ds_read_b128 v[84:87], v117 offset:36864\n\t"
        "s_waitcnt lgkmcnt(5)\n\t"
        "v_mfma_f32_32x32x16_bf16 v[32:47], v[68:71], v[64:67], v[32:47]\n\t"
        "s_waitcnt lgkmcnt(4)\n\t"
        "v_mfma_f32_32x32x16_bf16 v[48:63], v[72:75], v[64:67], v[48:63]\n\t"
        "ds_read_b64_tr_b16 v[88:89], v121 offset:40960\n\t"
        "ds_read_b64_tr_b16 v[90:91], v121 offset:41984\n\t"
        "ds_read_b128 v[92:95], v118 offset:32768\n\t"
        "ds_read_b128 v[96:99], v118 offset:36864\n\t"
        "s_waitcnt lgkmcnt(5)\n\t"
        "v_mfma_f32_32x32x16_bf16 v[32:47], v[80:83], v[76:79], v[32:47]\n\t"
        "s_waitcnt lgkmcnt(4)\n\t"
        "v_mfma_f32_32x32x16_bf16 v[48:63], v[84:87], v[76:79], v[48:63]\n\t"
        "s_waitcnt vmcnt(4)\n\t"
        "s_barrier\n\t"
        "s_add_u32 m0, s26, 0\n\t"
        "s_nop 0\n\t"
        "global_load_lds_dwordx4 v122, s[22:23]\n\t"
        "global_load_lds_dwordx4 v123, s[22:23] offset:1024\n\t"
        "s_add_u32 m0, s26, 16384\n\t"
        "s_nop 0\n\t"
        "global_load_lds_dwordx4 v124, s[24:25]\n\t"
        "global_load_lds_dwordx4 v125, s[24:25] offset:1024\n\t"
        "s_add_u32 s22, s22, 128\n\t"
        "s_addc_u32 s23, s23, 0\n\t"
        "s_add_u32 s24, s24, 221184\n\t"
        "s_addc_u32 s25, s25, 0\n\t"
        "ds_read_b64_tr_b16 v[100:101], v121 offset:45056\n\t"
        "ds_read_b64_tr_b16 v[102:103], v121 offset:46080\n\t"
        "ds_read_b128 v[104:107], v119 offset:32768\n\t"
        "ds_read_b128 v[108:111], v119 offset:36864\n\t"
        "s_waitcnt lgkmcnt(5)\n\t"
        "v_mfma_f32_32x32x16_bf16 v[32:47], v[92:95], v[88:91], v[32:47]\n\t"
        "s_waitcnt lgkmcnt(4)\n\t"
        "v_mfma_f32_32x32x16_bf16 v[48:63], v[96:99], v[88:91], v[48:63]\n\t"
        "ds_read_b64_tr_b16 v[64:65], v121 offset:0\n\t"
        "ds_read_b64_tr_b16 v[66:67], v121 offset:1024\n\t"
        "ds_read_b128 v[68:71], v116 offset:0\n\t"
        "ds_read_b128 v[72:75], v116 offset:4096\n\t"
        "ds_read_u16_d16_hi v208, v200 offset:0\n\t"
        "ds_read_u16_d16_hi v209, v200 offset:144\n\t"
        "ds_read_u16_d16_hi v210, v200 offset:288\n\t"
        "ds_read_u16_d16_hi v211, v200 offset:432\n\t"
        "ds_read_u16_d16_hi v212, v200 offset:10240\n\t"
        "ds_read_u16_d16_hi v213, v201 offset:6\n\t"
        "ds_read_u16_d16_hi v214, v201 offset:4\n\t"
        "ds_read_u16_d16_hi v215, v201 offset:2\n\t"
        "ds_read_u16_d16_hi v216, v201 offset:0\n\t"
        "s_waitcnt lgkmcnt(14)\n\t"
        "v_mfma_f32_32x32x16_bf16 v[32:47], v[104:107], v[100:103], v[32:47]\n\t"
        "s_waitcnt lgkmcnt(13)\n\t"
        "v_mfma_f32_32x32x16_bf16 v[48:63], v[108:111], v[100:103], v[48:63]\n\t"
        "ds_read_b64_tr_b16 v[76:77], v121 offset:4096\n\t"
        "ds_read_b64_tr_b16 v[78:79], v121 offset:5120\n\t"
        "ds_read_b128 v[80:83], v117 offset:0\n\t"
        "ds_read_b128 v[84:87], v117 offset:4096\n\t"
        "s_waitcnt lgkmcnt(14)\n\t"
        "v_mfma_f32_32x32x16_bf16 v[32:47], v[68:71], v[64:67], v[32:47]\n\t"
        "s_waitcnt lgkmcnt(13)\n\t"
        "v_mfma_f32_32x32x16_bf16 v[48:63], v[72:75], v[64:67], v[48:63]\n\t"
        "s_waitcnt lgkmcnt(4)\n\t"
        "v_mul_f32 v213, v208, v213\n\t"
        "v_fmac_f32 v213, v209, v214\n\t"
        "v_fmac_f32 v213, v210, v215\n\t"
        "v_fmac_f32 v213, v211, v216\n\t"
        "v_mul_f32 v213, v213, v212\n\t"
        "v_cvt_pk_bf16_f32 v213, v213, v213\n\t"
        "ds_write_b16 v202, v213\n\t"
        "ds_read_b64_tr_b16 v[88:89], v121 offset:8192\n\t"
        "ds_read_b64_tr_b16 v[90:91], v121 offset:9216\n\t"
        "ds_read_b128 v[92:95], v118 offset:0\n\t"
        "ds_read_b128 v[96:99], v118 offset:4096\n\t"
        "s_waitcnt lgkmcnt(6)\n\t"
        "v_mfma_f32_32x32x16_bf16 v[32:47], v[80:83], v[76:79], v[32:47]\n\t"
        "s_waitcnt lgkmcnt(5)\n\t"
        "v_mfma_f32_32x32x16_bf16 v[48:63], v[84:87], v[76:79], v[48:63]\n\t"
        "s_waitcnt vmcnt(4)\n\t"
        "s_barrier\n\t"
        "s_add_u32 m0, s26, 98304\n\t"
        "s_nop 0\n\t"
        "global_load_lds_dwordx4 v122, s[22:23]\n\t"
        "global_load_lds_dwordx4 v123, s[22:23] offset:1024\n\t"
        "s_add_u32 m0, s26, 114688\n\t"
        "s_nop 0\n\t"
        "global_load_lds_dwordx4 v124, s[24:25]\n\t"
        "global_load_lds_dwordx4 v125, s[24:25] offset:1024\n\t"
        "s_add_u32 s22, s22, 128\n\t"
        "s_addc_u32 s23, s23, 0\n\t"
        "s_add_u32 s24, s24, 221184\n\t"
        "s_addc_u32 s25, s25, 0\n\t"
        "ds_read_b64_tr_b16 v[100:101], v121 offset:12288\n\t"
        "ds_read_b64_tr_b16 v[102:103], v121 offset:13312\n\t"
        "ds_read_b128 v[104:107], v119 offset:0\n\t"
        "ds_read_b128 v[108:111], v119 offset:4096\n\t"
        "s_waitcnt lgkmcnt(5)\n\t"
        "v_mfma_f32_32x32x16_bf16 v[32:47], v[92:95], v[88:91], v[32:47]\n\t"
        "s_waitcnt lgkmcnt(4)\n\t"
        "v_mfma_f32_32x32x16_bf16 v[48:63], v[96:99], v[88:91], v[48:63]\n\t"
        "ds_read_b64_tr_b16 v[64:65], v120 offset:32768\n\t"
        "ds_read_b64_tr_b16 v[66:67], v120 offset:33792\n\t"
        "ds_read_b128 v[68:71], v112 offset:32768\n\t"
        "ds_read_b128 v[72:75], v112 offset:36864\n\t"
        "s_waitcnt lgkmcnt(5)\n\t"
        "v_mfma_f32_32x32x16_bf16 v[32:47], v[104:107], v[100:103], v[32:47]\n\t"
        "s_waitcnt lgkmcnt(4)\n\t"
        "v_mfma_f32_32x32x16_bf16 v[48:63], v[108:111], v[100:103], v[48:63]\n\t"
        "ds_read_b64_tr_b16 v[76:77], v120 offset:36864\n\t"
        "ds_read_b64_tr_b16 v[78:79], v120 offset:37888\n\t"
        "ds_read_b128 v[80:83], v113 offset:32768\n\t"
        "ds_read_b128 v[84:87], v113 offset:36864\n\t"
        "s_waitcnt lgkmcnt(5)\n\t"
        "v_mfma_f32_32x32x16_bf16 v[32:47], v[68:71], v[64:67], v[32:47]\n\t"
        "s_waitcnt lgkmcnt(4)\n\t"
        "v_mfma_f32_32x32x16_bf16 v[48:63], v[72:75], v[64:67], v[48:63]\n\t"
        "ds_read_b64_tr_b16 v[88:89], v120 offset:40960\n\t"
        "ds_read_b64_tr_b16 v[90:91], v120 offset:41984\n\t"
        "ds_read_b128 v[92:95], v114 offset:32768\n\t"
        "ds_read_b128 v[96:99], v114 offset:36864\n\t"
        "s_waitcnt lgkmcnt(5)\n\t"
        "v_mfma_f32_32x32x16_bf16 v[32:47], v[80:83], v[76:79], v[32:47]\n\t"
        "s_waitcnt lgkmcnt(4)\n\t"
        "v_mfma_f32_32x32x16_bf16 v[48:63], v[84:87], v[76:79], v[48:63]\n\t"
        "s_waitcnt vmcnt(4)\n\t"
        "s_barrier\n\t"
        "v_add_co_u32 v196, vcc, 1, v196\n\t"
        "s_nop 1\n\t"
        "v_addc_co_u32 v197, vcc, 0, v197, vcc\n\t"
        "s_add_u32 m0, s26, 65536\n\t"
        "s_nop 0\n\t"
        "global_load_lds_dwordx4 v122, s[22:23]\n\t"
        "global_load_lds_dwordx4 v123, s[22:23] offset:1024\n\t"
        "s_add_u32 m0, s26, 81920\n\t"
        "s_nop 0\n\t"
        "global_load_lds_dwordx4 v124, s[24:25]\n\t"
        "global_load_lds_dwordx4 v125, s[24:25] offset:1024\n\t"
        "s_add_u32 s22, s22, 128\n\t"
        "s_addc_u32 s23, s23, 0\n\t"
        "s_add_u32 s24, s24, 221184\n\t"
        "s_addc_u32 s25, s25, 0\n\t"
        "ds_read_b64_tr_b16 v[100:101], v120 offset:45056\n\t"
        "ds_read_b64_tr_b16 v[102:103], v120 offset:46080\n\t"
        "ds_read_b128 v[104:107], v115 offset:32768\n\t"
        "ds_read_b128 v[108:111], v115 offset:36864\n\t"
        "s_waitcnt lgkmcnt(5)\n\t"
        "v_mfma_f32_32x32x16_bf16 v[32:47], v[92:95], v[88:91], v[32:47]\n\t"
        "s_waitcnt lgkmcnt(4)\n\t"
        "v_mfma_f32_32x32x16_bf16 v[48:63], v[96:99], v[88:91], v[48:63]\n\t"
        "ds_read_b64_tr_b16 v[64:65], v120 offset:0\n\t"
        "ds_read_b64_tr_b16 v[66:67], v120 offset:1024\n\t"
        "ds_read_b128 v[68:71], v112 offset:0\n\t"
        "ds_read_b128 v[72:75], v112 offset:4096\n\t"
        "ds_read2_b32 v[128:129], v187 offset0:0 offset1:1\n\t"
        "ds_read2_b32 v[130:131], v187 offset0:2 offset1:3\n\t"
        "ds_read2_b32 v[132:133], v188 offset0:0 offset1:1\n\t"
        "ds_read2_b32 v[134:135], v188 offset0:2 offset1:3\n\t"
        "s_waitcnt lgkmcnt(9)\n\t"
        "v_mfma_f32_32x32x16_bf16 v[32:47], v[104:107], v[100:103], v[32:47]\n\t"
        "s_waitcnt lgkmcnt(8)\n\t"
        "v_mfma_f32_32x32x16_bf16 v[48:63], v[108:111], v[100:103], v[48:63]\n\t"
        "s_waitcnt lgkmcnt(0)\n\t"
        "buffer_store_dwordx4 v[128:131], v189, %[rs], 0 offen sc1\n\t"
        "buffer_store_dwordx4 v[132:135], v190, %[rs], 0 offen sc1\n\t"
        "ds_read_b64_tr_b16 v[76:77], v120 offset:4096\n\t"
        "ds_read_b64_tr_b16 v[78:79], v120 offset:5120\n\t"
        "ds_read_b128 v[80:83], v113 offset:0\n\t"
        "ds_read_b128 v[84:87], v113 offset:4096\n\t"
        "v_mfma_f32_32x32x16_bf16 v[32:47], v[68:71], v[64:67], v[32:47]\n\t"
        "v_mfma_f32_32x32x16_bf16 v[48:63], v[72:75], v[64:67], v[48:63]\n\t"
        "ds_read_b64_tr_b16 v[88:89], v120 offset:8192\n\t"
        "ds_read_b64_tr_b16 v[90:91], v120 offset:9216\n\t"
        "ds_read_b128 v[92:95], v114 offset:0\n\t"
        "ds_read_b128 v[96:99], v114 offset:4096\n\t"
        "s_waitcnt lgkmcnt(5)\n\t"
        "v_mfma_f32_32x32x16_bf16 v[32:47], v[80:83], v[76:79], v[32:47]\n\t"
        "s_waitcnt lgkmcnt(4)\n\t"
        "v_mfma_f32_32x32x16_bf16 v[48:63], v[84:87], v[76:79], v[48:63]\n\t"
        "s_waitcnt vmcnt(6)\n\t"
        "s_barrier\n\t"
        "s_add_u32 m0, s26, 32768\n\t"
        "s_nop 0\n\t"
        "global_load_lds_dwordx4 v122, s[22:23]\n\t"
        "global_load_lds_dwordx4 v123, s[22:23] offset:1024\n\t"
        "s_add_u32 m0, s26, 49152\n\t"
        "s_nop 0\n\t"
        "global_load_lds_dwordx4 v124, s[24:25]\n\t"
        "global_load_lds_dwordx4 v125, s[24:25] offset:1024\n\t"
        "s_add_u32 s22, s22, 128\n\t"
        "s_addc_u32 s23, s23, 0\n\t"
        "s_add_u32 s24, s24, 221184\n\t"
        "s_addc_u32 s25, s25, 0\n\t"
        "ds_read_b64_tr_b16 v[100:101], v120 offset:12288\n\t"
        "ds_read_b64_tr_b16 v[102:103], v120 offset:13312\n\t"
        "ds_read_b128 v[104:107], v115 offset:0\n\t"
        "ds_read_b128 v[108:111], v115 offset:4096\n\t"
        "s_waitcnt lgkmcnt(5)\n\t"
        "v_mfma_f32_32x32x16_bf16 v[32:47], v[92:95], v[88:91], v[32:47]\n\t"
        "s_waitcnt lgkmcnt(4)\n\t"
        "v_mfma_f32_32x32x16_bf16 v[48:63], v[96:99], v[88:91], v[48:63]\n\t"
        "ds_read_b64_tr_b16 v[64:65], v121 offset:32768\n\t"
        "ds_read_b64_tr_b16 v[66:67], v121 offset:33792\n\t"
        "ds_read_b128 v[68:71], v116 offset:32768\n\t"
        "ds_read_b128 v[72:75], v116 offset:36864\n\t"
        "s_waitcnt lgkmcnt(5)\n\t"
        "v_mfma_f32_32x32x16_bf16 v[32:47], v[104:107], v[100:103], v[32:47]\n\t"
        "s_waitcnt lgkmcnt(4)\n\t"
        "v_mfma_f32_32x32x16_bf16 v[48:63], v[108:111], v[100:103], v[48:63]\n\t"
        "ds_read_b64_tr_b16 v[76:77], v121 offset:36864\n\t"
        "ds_read_b64_tr_b16 v[78:79], v121 offset:37888\n\t"
        "ds_read_b128 v[80:83], v117 offset:32768\n\t"
        "ds_read_b128 v[84:87], v117 offset:36864\n\t"
        "s_waitcnt lgkmcnt(5)\n\t"
        "v_mfma_f32_32x32x16_bf16 v[32:47], v[68:71], v[64:67], v[32:47]\n\t"
        "s_waitcnt lgkmcnt(4)\n\t"
        "v_mfma_f32_32x32x16_bf16 v[48:63], v[72:75], v[64:67], v[48:63]\n\t"
        "ds_read_b64_tr_b16 v[88:89], v121 offset:40960\n\t"
        "ds_read_b64_tr_b16 v[90:91], v121 offset:41984\n\t"
        "ds_read_b128 v[92:95], v118 offset:32768\n\t"
        "ds_read_b128 v[96:99], v118 offset:36864\n\t"
        "s_waitcnt lgkmcnt(5)\n\t"
        "v_mfma_f32_32x32x16_bf16 v[32:47], v[80:83], v[76:79], v[32:47]\n\t"
        "s_waitcnt lgkmcnt(4)\n\t"
        "v_mfma_f32_32x32x16_bf16 v[48:63], v[84:87], v[76:79], v[48:63]\n\t"
        "s_waitcnt vmcnt(4)\n\t"
        "s_barrier\n\t"
        "s_add_u32 m0, s26, 0\n\t"
        "s_nop 0\n\t"
        "global_load_lds_dwordx4 v122, s[22:23]\n\t"
        "global_load_lds_dwordx4 v123, s[22:23] offset:1024\n\t"
        "s_add_u32 m0, s26, 16384\n\t"
        "s_nop 0\n\t"
        "global_load_lds_dwordx4 v124, s[24:25]\n\t"
        "global_load_lds_dwordx4 v125, s[24:25] offset:1024\n\t"
        "s_add_u32 s22, s22, 128\n\t"
        "s_addc_u32 s23, s23, 0\n\t"
        "s_add_u32 s24, s24, 221184\n\t"
        "s_addc_u32 s25, s25, 0\n\t"
        "s_cmp_lg_u32 %[wv], 0\n\t"
        "s_cbranch_scc1 2f\n\t"
        "s_mov_b64 s[28:29], exec\n\t"
        "s_mov_b64 exec, 1\n\t"
        "global_store_dwordx2 v191, v[196:197], %[epb] sc1\n\t"
        "s_mov_b64 exec, s[28:29]\n\t"
        "2:\n\t"
        "ds_read_b64_tr_b16 v[100:101], v121 offset:45056\n\t"
        "ds_read_b64_tr_b16 v[102:103], v121 offset:46080\n\t"
        "ds_read_b128 v[104:107], v119 offset:32768\n\t"
        "ds_read_b128 v[108:111], v119 offset:36864\n\t"
        "s_waitcnt lgkmcnt(5)\n\t"
        "v_mfma_f32_32x32x16_bf16 v[32:47], v[92:95], v[88:91], v[32:47]\n\t"
        "s_waitcnt lgkmcnt(4)\n\t"
        "v_mfma_f32_32x32x16_bf16 v[48:63], v[96:99], v[88:91], v[48:63]\n\t"
        "ds_read_b64_tr_b16 v[64:65], v121 offset:0\n\t"
        "ds_read_b64_tr_b16 v[66:67], v121 offset:1024\n\t"
        "ds_read_b128 v[68:71], v116 offset:0\n\t"
        "ds_read_b128 v[72:75], v116 offset:4096\n\t"
        "s_waitcnt lgkmcnt(5)\n\t"
        "v_mfma_f32_32x32x16_bf16 v[32:47], v[104:107], v[100:103], v[32:47]\n\t"
        "s_waitcnt lgkmcnt(4)\n\t"
        "v_mfma_f32_32x32x16_bf16 v[48:63], v[108:111], v[100:103], v[48:63]\n\t"
        "ds_read_b64_tr_b16 v[76:77], v121 offset:4096\n\t"
        "ds_read_b64_tr_b16 v[78:79], v121 offset:5120\n\t"
        "ds_read_b128 v[80:83], v117 offset:0\n\t"
        "ds_read_b128 v[84:87], v117 offset:4096\n\t"
        "s_waitcnt lgkmcnt(5)\n\t"
        "v_mfma_f32_32x32x16_bf16 v[32:47], v[68:71], v[64:67], v[32:47]\n\t"
        "s_waitcnt lgkmcnt(4)\n\t"
        "v_mfma_f32_32x32x16_bf16 v[48:63], v[72:75], v[64:67], v[48:63]\n\t"
        "ds_read_b64_tr_b16 v[88:89], v121 offset:8192\n\t"
        "ds_read_b64_tr_b16 v[90:91], v121 offset:9216\n\t"
        "ds_read_b128 v[92:95], v118 offset:0\n\t"
        "ds_read_b128 v[96:99], v118 offset:4096\n\t"
        "s_waitcnt lgkmcnt(5)\n\t"
        "v_mfma_f32_32x32x16_bf16 v[32:47], v[80:83], v[76:79], v[32:47]\n\t"
        "s_waitcnt lgkmcnt(4)\n\t"
        "v_mfma_f32_32x32x16_bf16 v[48:63], v[84:87], v[76:79], v[48:63]\n\t"
        "s_waitcnt vmcnt(4)\n\t"
        "s_barrier\n\t"
        "s_mov_b32 m0, s48\n\t"
        "s_nop 0\n\t"
        "global_load_lds_dwordx4 %[l16], s[44:45]\n\t"
        "global_load_lds_dwordx4 %[l16], s[44:45] offset:1024\n\t"
        "global_load_lds_dwordx4 %[l16], s[44:45] offset:2048\n\t"
        "s_lshr_b64 exec, exec, 1\n\t"
        "global_load_lds_dwordx4 %[l16], s[44:45] offset:3072\n\t"
        "s_mov_b64 exec, -1\n\t"
        "ds_read_b64_tr_b16 v[100:101], v121 offset:12288\n\t"
        "ds_read_b64_tr_b16 v[102:103], v121 offset:13312\n\t"
        "ds_read_b128 v[104:107], v119 offset:0\n\t"
        "ds_read_b128 v[108:111], v119 offset:4096\n\t"
        "s_waitcnt lgkmcnt(5)\n\t"
        "v_mfma_f32_32x32x16_bf16 v[32:47], v[92:95], v[88:91], v[32:47]\n\t"
        "s_waitcnt lgkmcnt(4)\n\t"
        "v_mfma_f32_32x32x16_bf16 v[48:63], v[96:99], v[88:91], v[48:63]\n\t"
        "ds_read_b64_tr_b16 v[64:65], v120 offset:32768\n\t"
        "ds_read_b64_tr_b16 v[66:67], v120 offset:33792\n\t"
        "ds_read_b128 v[68:71], v112 offset:32768\n\t"
        "ds_read_b128 v[72:75], v112 offset:36864\n\t"
        "s_waitcnt lgkmcnt(5)\n\t"
        "v_mfma_f32_32x32x16_bf16 v[32:47], v[104:107], v[100:103], v[32:47]\n\t"
        "s_waitcnt lgkmcnt(4)\n\t"
        "v_mfma_f32_32x32x16_bf16 v[48:63], v[108:111], v[100:103], v[48:63]\n\t"
        "ds_read_b64_tr_b16 v[76:77], v120 offset:36864\n\t"
        "ds_read_b64_tr_b16 v[78:79], v120 offset:37888\n\t"
        "ds_read_b128 v[80:83], v113 offset:32768\n\t"
        "ds_read_b128 v[84:87], v113 offset:36864\n\t"
        "s_waitcnt lgkmcnt(5)\n\t"
        "v_mfma_f32_32x32x16_bf16 v[32:47], v[68:71], v[64:67], v[32:47]\n\t"
        "s_waitcnt lgkmcnt(4)\n\t"
        "v_mfma_f32_32x32x16_bf16 v[48:63], v[72:75], v[64:67], v[48:63]\n\t"
        "ds_read_b64_tr_b16 v[88:89], v120 offset:40960\n\t"
        "ds_read_b64_tr_b16 v[90:91], v120 offset:41984\n\t"
        "ds_read_b128 v[92:95], v114 offset:32768\n\t"
        "ds_read_b128 v[96:99], v114 offset:36864\n\t"
        "s_waitcnt lgkmcnt(5)\n\t"
        "v_mfma_f32_32x32x16_bf16 v[32:47], v[80:83], v[76:79], v[32:47]\n\t"
        "s_waitcnt lgkmcnt(4)\n\t"
        "v_mfma_f32_32x32x16_bf16 v[48:63], v[84:87], v[76:79], v[48:63]\n\t"
        "s_waitcnt vmcnt(4)\n\t"
        "s_barrier\n\t"
        "s_mov_b32 m0, s49\n\t"
        "s_nop 0\n\t"
        "global_load_lds_dwordx4 %[l16], s[46:47]\n\t"
        "s_mov_b64 exec, s[50:51]\n\t"
        "global_load_lds_dwordx4 %[l16], s[46:47] offset:1024\n\t"
        "s_mov_b64 exec, -1\n\t"
        "ds_read_b64_tr_b16 v[100:101], v120 offset:45056\n\t"
        "ds_read_b64_tr_b16 v[102:103], v120 offset:46080\n\t"
        "ds_read_b128 v[104:107], v115 offset:32768\n\t"
        "ds_read_b128 v[108:111], v115 offset:36864\n\t"
        "s_waitcnt lgkmcnt(5)\n\t"
        "v_mfma_f32_32x32x16_bf16 v[32:47], v[92:95], v[88:91], v[32:47]\n\t"
        "s_waitcnt lgkmcnt(4)\n\t"
        "v_mfma_f32_32x32x16_bf16 v[48:63], v[96:99], v[88:91], v[48:63]\n\t"
        "ds_read_b64_tr_b16 v[64:65], v120 offset:0\n\t"
        "ds_read_b64_tr_b16 v[66:67], v120 offset:1024\n\t"
        "ds_read_b128 v[68:71], v112 offset:0\n\t"
        "ds_read_b128 v[72:75], v112 offset:4096\n\t"
        "s_waitcnt lgkmcnt(5)\n\t"
        "v_mfma_f32_32x32x16_bf16 v[32:47], v[104:107], v[100:103], v[32:47]\n\t"
        "s_waitcnt lgkmcnt(4)\n\t"
        "v_mfma_f32_32x32x16_bf16 v[48:63], v[108:111], v[100:103], v[48:63]\n\t"
        "ds_read_b64_tr_b16 v[76:77], v120 offset:4096\n\t"
        "ds_read_b64_tr_b16 v[78:79], v120 offset:5120\n\t"
        "ds_read_b128 v[80:83], v113 offset:0\n\t"
        "ds_read_b128 v[84:87], v113 offset:4096\n\t"
        "s_waitcnt lgkmcnt(5)\n\t"
        "v_mfma_f32_32x32x16_bf16 v[32:47], v[68:71], v[64:67], v[32:47]\n\t"
        "s_waitcnt lgkmcnt(4)\n\t"
        "v_mfma_f32_32x32x16_bf16 v[48:63], v[72:75], v[64:67], v[48:63]\n\t"
        "ds_read_b64_tr_b16 v[88:89], v120 offset:8192\n\t"
        "ds_read_b64_tr_b16 v[90:91], v120 offset:9216\n\t"
        "ds_read_b128 v[92:95], v114 offset:0\n\t"
        "ds_read_b128 v[96:99], v114 offset:4096\n\t"
        "s_waitcnt lgkmcnt(5)\n\t"
        "v_mfma_f32_32x32x16_bf16 v[32:47], v[80:83], v[76:79], v[32:47]\n\t"
        "s_waitcnt lgkmcnt(4)\n\t"
        "v_mfma_f32_32x32x16_bf16 v[48:63], v[84:87], v[76:79], v[48:63]\n\t"
        "ds_read_b64_tr_b16 v[100:101], v120 offset:12288\n\t"
        "ds_read_b64_tr_b16 v[102:103], v120 offset:13312\n\t"
        "ds_read_b128 v[104:107], v115 offset:0\n\t"
        "ds_read_b128 v[108:111], v115 offset:4096\n\t"
        "s_waitcnt lgkmcnt(5)\n\t"
        "v_mfma_f32_32x32x16_bf16 v[32:47], v[92:95], v[88:91], v[32:47]\n\t"
        "s_waitcnt lgkmcnt(4)\n\t"
        "v_mfma_f32_32x32x16_bf16 v[48:63], v[96:99], v[88:91], v[48:63]\n\t"
        "s_waitcnt lgkmcnt(1)\n\t"
        "v_mfma_f32_32x32x16_bf16 v[32:47], v[104:107], v[100:103], v[32:47]\n\t"
        "s_waitcnt lgkmcnt(0)\n\t"
        "v_mfma_f32_32x32x16_bf16 v[48:63], v[108:111], v[100:103], v[48:63]\n\t"
        "s_mov_b32 m0, s21\n\t"
        "s_nop 15\n\t"
        "s_nop 3\n\t"
        "19:\n\t"
            : "=&{v[32:47]}"(acc[0]), "=&{v[48:63]}"(acc[1]), [uok] "=&s"(uok), "=&{v[192:199]}"(misc)
            P1_INS
            :
          "v64", "v65", "v66", "v67", "v68", "v69", "v70", "v71", "v72", "v73", "v74", "v75", "v76", "v77", "v78", "v79", "v80", "v81", "v82", "v83", "v84", "v85", "v86", "v87", "v88", "v89", "v90", "v91", "v92", "v93", "v94", "v95", "v96", "v97", "v98", "v99", "v100", "v101", "v102", "v103", "v104", "v105", "v106", "v107", "v108", "v109", "v110", "v111", "v116", "v117", "v118", "v119", "v120", "v121", "v126", "v127", "v128", "v129", "v130", "v131", "v132", "v133", "v134", "v135", "v136", "v137", "v138", "v139", "v140", "v141", "v142", "v143", "v144", "v145", "v146", "v147", "v148", "v149", "v150", "v151", "v152", "v153", "v154", "v155", "v156", "v157", "v158", "v159", "v160", "v161", "v162", "v163", "v164", "v165", "v166", "v167", "v168", "v169", "v170", "v171", "v172", "v173", "v174", "v175", "v176", "v177", "v178", "v179", "v180", "v181", "v182", "v183", "v208", "v209", "v210", "v211", "v212", "v213", "v214", "v215", "v216", "s21", "s22", "s23", "s24", "s25", "s26", "s27", "s28", "s29", "s30", "s31", "scc", "vcc", "memory"
        );
    }
    Carry cy;
    cy.tag = ((u64)misc[5] << 32) | misc[4];
    cy.dv = misc[6];
    const u64 tag = cy.tag;
    u32 eo[4] = {eo0, 0, 0, 0}, es[2] = {0, 0}, ed[3] = {0, 0, 0}, er = 0;
    float esc[2] = {1.f, 1.f};
    const bool anyv = 3 * cg + 2 >= 32;
    if (ft == 3) {
        const u32 stgw = lds + LEFT_ST + th * 1536;
        u32 D[3], S[3], VD[3];
#pragma unroll
        for (int bi = 0; bi < 3; ++bi) {
            const int bb = 3 * cg + bi, tyb = bb >> 4, hb = 2 * (bb & 15);
            const u32 kvb = WS_KV + hb * KVIMG + tb * CH;
            D[bi] = tyb == 2 ? OOB : (tyb == 0 ? WS_QL + (hb * SEQ + tb * 128) * 4 : kvb + 4096) + th * 256;
            S[bi] = tyb == 0 ? SEQ * 4 : KVIMG;
            VD[bi] = tyb == 2 ? kvb + 4608 + 4 * th * VB + 512 : OOB;
        }
        eo[0] = (hh ? D[1] : D[0]) + r * 8;
        eo[1] = eo[0] + (hh ? S[1] : S[0]);
        eo[2] = hh ? OOB : D[2] + r * 8;
        eo[3] = hh ? OOB : eo[2] + S[2];
        esc[0] = 3 * cg + hh < 16 ? QSCALE : 1.f;
        esc[1] = 3 * cg + 2 < 16 ? QSCALE : 1.f;
        if (anyv) {
            es[0] = 3 * cg + hh >= 32 ? stgw + hh * 512 + r * 2 : OOR;
            es[1] = hh ? OOR : stgw + 1024 + r * 2;
            const u32 xl = hh * KVIMG + ((lane >> 3) & 3) * VB + (lane & 7) * 16;
#pragma unroll
            for (int s = 0; s < 3; ++s) ed[s] = VD[s] + xl;
            er = (lane & 1) ? OOR : stgw + hh * 256 + ((lane >> 2) & 1) * 128 + (2 * ((lane >> 3) & 3) + ((lane >> 1) & 1)) * 16;
        } else { es[0] = es[1] = OOR; }
    }
    if (ft == 3) {
#pragma unroll
        for (int q = 0; q < 2; ++q)
#pragma unroll
            for (int fh = 0; fh < 2; ++fh) {
                const u32 w0 = pk2(acc[0][4 * q + 2 * fh] * esc[q], acc[0][4 * q + 2 * fh + 1] * esc[q]);
                const u32 w1 = pk2(acc[1][4 * q + 2 * fh] * esc[q], acc[1][4 * q + 2 * fh + 1] * esc[q]);
                const u32x2 v = {w0, w1};
                __builtin_amdgcn_raw_buffer_store_b64(v, ws, eo[2 * q + fh], 0, SC1);
                lds_st<u16>(es[q] + fh * 256, (u16)w0);
                lds_st<u16>(es[q] + fh * 256 + 128, (u16)(w0 >> 16));
                lds_st<u16>(es[q] + fh * 256 + 64, (u16)w1);
                lds_st<u16>(es[q] + fh * 256 + 192, (u16)(w1 >> 16));
            }
        if (anyv) {
            const u32 one = (lane & 1) ? 0x3f803f80u : 0u;
#pragma unroll
            for (int s = 0; s < 3; ++s) {
                u32x4 a = lds_ld<u32x4>(er + s * 512);
#pragma unroll
                for (int j = 0; j < 4; ++j) a[j] |= one;
                st16(ws, ed[s], a);
            }
        }
    } else {
      asm volatile("" ::: "memory");
      const u32x2 f0v = __builtin_amdgcn_raw_buffer_load_b64(ws, WS_F + f0p, 0, SC1);
      asm volatile("" ::: "memory");
      u32x4 sv[4];
      u32 sa[4];
      if (ty == 2) {
#pragma unroll
        for (int i = 0; i < 2; ++i)
#pragma unroll
            for (int bh = 0; bh < 2; ++bh) {
                const f32x16 a = acc[i];
                const auto x0 = __builtin_amdgcn_permlane32_swap(pk2(a[8 * bh], a[8 * bh + 1]), pk2(a[8 * bh + 4], a[8 * bh + 5]), false, false);
                const auto x1 = __builtin_amdgcn_permlane32_swap(pk2(a[8 * bh + 2], a[8 * bh + 3]), pk2(a[8 * bh + 6], a[8 * bh + 7]), false, false);
                const u32x4 v = {x0[0], x1[0], x0[1], x1[1]};
                sv[2 * i + bh] = v; sa[2 * i + bh] = eo[0] + (2 * i + bh) * VB;
            }
      } else {
        const float sc = ty == 0 ? QSCALE : 1.f;
        const u32 hs = ty == 0 ? 65536 : KVIMG;
#pragma unroll
        for (int i = 0; i < 2; ++i)
#pragma unroll
            for (int fh = 0; fh < 2; ++fh) {
                const f32x16 a = acc[i];
                u32x4 v;
#pragma unroll
                for (int j = 0; j < 4; ++j) v[j] = pk2(a[8 * fh + 2 * j] * sc, a[8 * fh + 2 * j + 1] * sc);
                sv[2 * i + fh] = v; sa[2 * i + fh] = eo[0] + fh * hs + i * 1024;
            }
      }
#pragma unroll
      for (int k = 0; k < 4; ++k) st16(ws, sa[k], sv[k]);
        asm volatile("" ::: "memory");
        if (!__all((((u64)f0v[1] << 32) | f0v[0]) == tag)) wait_flags(fb, f0p, tag);
    }
    {
        const int i6 = ft == 3 ? 0 : w - th, uc = i6 >> 1;
        const u32 so = ft == 3 ? OOB : WS_UT + conv_chan(b, uc) * UROW + (i6 & 1) * 2048 + lane * 16;
        cy.uda = ft == 3 ? OOR : lds + conv_area(uc) + 4 * HC + (i6 & 1) * 2048 + lane * 16;
#pragma unroll
        for (int j = 0; j < 2; ++j) { cy.ua[j] = ld16(ws, so + j * 1024); cy.ub[j] = ld16(ws, so + j * 1024 + 4); }
        asm volatile("" ::: "memory");
    }
    {
        u32 fa = OOR;
        u32x4 fv = {0, 0, 0, 0};
        if (w == 0) {
            const u32 ar = l4 == 0 ? CONV_A : (l4 == 1 ? CONV_B : CONV_C);
            fa = lane < 48 ? lds + ar + ((lane >> 2) & 3) * HC + c3 * 16 : OOR;
            const int k = (lane >> 2) & 3;
#pragma unroll
            for (int e = 0; e < 4; ++e) {
                const int s = e - 3 + k;
                fv[e] = (c3 != 3 || s < 0) ? 0u : (s == 0 ? misc[0] : (s == 1 ? misc[1] : (s == 2 ? misc[2] : misc[3])));
            }
        } else if (w < 4) {
            fa = r < 28 ? lds + conv_area(w - 1) + 4 * HC + (hh ? 0 : UC + 32) + (256 + r) * 16 : OOR;
        }
        lds_st<u32x4>(fa, fv);
    }
    return cy;
}

__device__ __forceinline__ bf16x8 conv_dfrag(float dval, int lane) {
    typedef short s8 __attribute__((ext_vector_type(8)));
    const int r = lane & 15, g = lane >> 4;
    const short db = (short)(__float_as_uint(dval) >> 16);
    s8 d;
#pragma unroll
    for (int j = 0; j < 8; ++j) d[j] = (8 * g + j == 30 - 2 * r) ? db : (short)0;
    return __builtin_bit_cast(bf16x8, d);
}

__device__ __forceinline__ void conv_quarter(u32 aa, u32 ba, int j, bf16x8 AD, f32x4& aH, f32x4& aL) {
    const u32 bH = ba - (7 - j) * 512, bL = ba - j * 512;
#pragma unroll
    for (int q = 0; q < 2; ++q) {
        bf16x8 A[8], BH[8], BL[4];
#pragma unroll
        for (int m = 0; m < 8; ++m) { A[m] = lds_ld<bf16x8>(aa + q * 64 + m * 512); BH[m] = lds_ld<bf16x8>(bH + q * 64 + m * 512); }
#pragma unroll
        for (int m = 0; m < 4; ++m) BL[m] = lds_ld<bf16x8>(bL + q * 64 + m * 512);
#pragma unroll
        for (int m = 0; m < 8; ++m) {
            if (m <= 7 - j) aH = __builtin_amdgcn_mfma_f32_16x16x32_bf16(A[m], BH[m], aH, 0, 0, 0);
            if (m < 4 && m <= j) aL = __builtin_amdgcn_mfma_f32_16x16x32_bf16(A[m], BL[m], aL, 0, 0, 0);
        }
        if (q == 0) {
            aH = __builtin_amdgcn_mfma_f32_16x16x32_bf16(AD, BH[0], aH, 0, 0, 0);
            aL = __builtin_amdgcn_mfma_f32_16x16x32_bf16(AD, BL[0], aL, 0, 0, 0);
        }
    }
}

__device__ __forceinline__ void conv_store2(rsrc_t ws, int ch, int nE, int nO, int lane, f32x4 aE, f32x4 aO) {
    const int c = lane & 15, g = lane >> 4;
    const bool odd = c & 1;
    u32x4 v;
#pragma unroll
    for (int j = 0; j < 4; ++j) {
        const float snd = odd ? aE[j] : aO[j], own = odd ? aO[j] : aE[j];
        const float rcv = __uint_as_float(__builtin_amdgcn_update_dpp(0u, __float_as_uint(snd), 0xB1, 0xf, 0xf, true));
        v[j] = odd ? pk2(rcv, own) : pk2(own, rcv);
    }
    st16(ws, WS_YT + ch * 4096 + (256 * (odd ? nO : nE) + 32 * (c >> 1) + 8 * g) * 2, v);
}

struct AttnSetup { u32 aK, aL, aV, qa, qla, hm, wf[5], wx[2], omo, olo; };

__device__ __forceinline__ AttnSetup attn_setup(int b, u32 lds) {
    const int lane = threadIdx.x & 63;
    const int w = uni(threadIdx.x >> 6);
    const int xcd = b & 7, ii = b >> 3;
    const int h = xcd * 4 + (ii >> 3), qb = ii & 7, qt = qb * 8 + w;
    const int rho = lane & 31, ha = lane >> 5;
    AttnSetup a;
    const bool ext = rho == 16 || rho == 17 || rho == 20 || rho == 21;
    a.aK = lds + lane * 16;
    a.aL = ha ? OOR : lds + rho * 8;
    a.aV = rho < 16 ? lds + ha * 256 + rho * 16 : (ext ? lds + 512 + ((rho >> 2) & 1) * 64 + ha * 32 + (rho & 1) * 16 : OOR);
    a.qa = lds + QST + w * 1024 + lane * 16;
    a.qla = lds + QST + 8192 + (w >> 1) * 256 + rho * 8 + (w & 1) * 4;
    a.hm = ha ? 0u : 0xffffffffu;
#pragma unroll
    for (int j = 0; j < 5; ++j) {
        const int idx = 64 * j + lane, P = (idx * 6554) >> 16, c = idx - 10 * P;
        a.wf[j] = P * 1152 + (c < 9 ? c : 8) * 16 + 2048 - 1024 * j;
    }
#pragma unroll
    for (int s = 0; s < 2; ++s) {
        const int e = (s == 1 && w < 2) ? 8 + w : w, G = 16 + (e >= 5), j = e - 5 * (e >= 5);
        const int idx = 64 * j + lane, P = (idx * 6554) >> 16, c = idx - 10 * P;
        a.wx[s] = (G * 32 + P) * 1152 + (c < 9 ? c : 8) * 16;
    }
    a.omo = WS_OM + (((2 * qt + (rho >> 4)) * 16 + (h >> 1)) * 64 + (rho & 15) + 16 * (2 * (h & 1) + ha)) * 16;
    a.olo = ha ? OOB : WS_OL + (h * SEQ + qt * 32 + rho) * 4;
    return a;
}

__device__ __forceinline__ void phase_conv(const Ptrs& p, rsrc_t ws, int b, u32 lds, const char* fb, const Carry& cy, AttnSetup& as) {
    const int tid = threadIdx.x, lane = tid & 63;
    const int w = uni(tid >> 6);
    const int j4 = b & 3;
    const int xcd = b & 7, ii = b >> 3;
    const int cc = w < 4 ? (w >> 1) : 2;
    const int ch = conv_chan(b, cc);
    const u64 tag = cy.tag;
    as = attn_setup(b, lds);
    const int rho0 = w < 4 ? 4 * (w & 1) : 2 * (w & 3);
    u32 caa, cba;
    {
        const int r = lane & 15, g = lane >> 4;
        const u32 base = lds + conv_area(cc);
        caa = base + (r & 3) * HC + ((r >> 2) + g) * 16 + rho0 * 64;
        cba = base + 4 * HC + ((r & 1) ? 0 : UC + 32) + (252 - 4 * (r >> 1) + g) * 16 + rho0 * 64;
    }
    bf16x8 cad = conv_dfrag(rho0 == 0 ? bflo(cy.dv) : 0.f, lane);
    asm volatile("" : "+v"(as.aK), "+v"(as.aL), "+v"(as.aV), "+v"(as.qa), "+v"(as.qla), "+v"(as.hm), "+v"(as.wf[0]), "+v"(as.wf[1]), "+v"(as.wf[2]),
                      "+v"(as.wf[3]), "+v"(as.wf[4]), "+v"(as.wx[0]), "+v"(as.wx[1]), "+v"(as.omo), "+v"(as.olo), "+v"(caa), "+v"(cba), "+v"(cad));
    const int h = xcd * 4 + (ii >> 3), qb = ii & 7, tg = 256 * qb + 8 * h + 2 * (w & 3);
    asm volatile("" ::: "memory");
#pragma unroll
    for (int j = 0; j < 2; ++j) {
        u32x4 o;
#pragma unroll
        for (int k = 0; k < 4; ++k) o[k] = __builtin_amdgcn_alignbit(cy.ub[j][k], cy.ua[j][k], 16);
        lds_st<u32x4>(cy.uda + j * 1024, cy.ua[j]);
        lds_st<u32x4>(cy.uda == OOR ? OOR : cy.uda + j * 1024 + UC + 32, o);
    }
    WAITV(0);
    BAR();
    st8(ws, tid == 0 ? WS_F + FL_F1 + (((xcd & 1) * 8 + (ii >> 2)) * 16 + (xcd >> 1) * 4 + (ii & 3)) * 8 : OOB, tag);
    u32x4 xa, ga, xb, gb, xc, xd;
    if (w >= 4)
        asm volatile("global_load_dwordx4 %0, %6, %8\n\tglobal_load_dwordx4 %1, %6, %9\n\tglobal_load_dwordx4 %2, %7, %8\n\t"
                     "global_load_dwordx4 %3, %7, %9\n\tglobal_load_dwordx4 %4, %6, %8 offset:1152\n\tglobal_load_dwordx4 %5, %7, %8 offset:1152"
                     : "=&v"(xa), "=&v"(ga), "=&v"(xb), "=&v"(gb), "=&v"(xc), "=&v"(xd)
                     : "v"(lane * 16), "v"(1024 + (lane & 7) * 16), "s"(uni64(p.x + (size_t)tg * DM)), "s"(uni64(p.gw))
                     : "memory");
    f32x4 acc[8];
    const u32 red = lds + CONV_RED;
    if (w < 4) {
        f32x16 lo, hi;
        asm volatile(
        "ds_read_b128 v[64:67], %[aa] offset:0\n\t"
        "ds_read_b128 v[124:127], %[ba] offset:0\n\t"
        "ds_read_b128 v[120:123], %[ba] offset:512\n\t"
        "ds_read_b128 v[116:119], %[ba] offset:1024\n\t"
        "ds_read_b128 v[68:71], %[aa] offset:512\n\t"
        "ds_read_b128 v[112:115], %[ba] offset:1536\n\t"
        "ds_read_b128 v[72:75], %[aa] offset:1024\n\t"
        "ds_read_b128 v[76:79], %[aa] offset:1536\n\t"
        "ds_read_b128 v[108:111], %[ba] offset:2048\n\t"
        "ds_read_b128 v[80:83], %[aa] offset:2048\n\t"
        "ds_read_b128 v[104:107], %[ba] offset:2560\n\t"
        "ds_read_b128 v[84:87], %[aa] offset:2560\n\t"
        "ds_read_b128 v[100:103], %[ba] offset:3072\n\t"
        "ds_read_b128 v[88:91], %[aa] offset:3072\n\t"
        "ds_read_b128 v[96:99], %[ba] offset:3584\n\t"
        "ds_read_b128 v[92:95], %[aa] offset:3584\n\t"
        "v_mov_b32 v32, 0\n\t"
        "v_mov_b32 v33, 0\n\t"
        "v_mov_b32 v34, 0\n\t"
        "v_mov_b32 v35, 0\n\t"
        "v_mov_b32 v36, 0\n\t"
        "v_mov_b32 v37, 0\n\t"
        "v_mov_b32 v38, 0\n\t"
        "v_mov_b32 v39, 0\n\t"
        "v_mov_b32 v40, 0\n\t"
        "v_mov_b32 v41, 0\n\t"
        "v_mov_b32 v42, 0\n\t"
        "v_mov_b32 v43, 0\n\t"
        "v_mov_b32 v44, 0\n\t"
        "v_mov_b32 v45, 0\n\t"
        "v_mov_b32 v46, 0\n\t"
        "v_mov_b32 v47, 0\n\t"
        "v_mov_b32 v48, 0\n\t"
        "v_mov_b32 v49, 0\n\t"
        "v_mov_b32 v50, 0\n\t"
        "v_mov_b32 v51, 0\n\t"
        "v_mov_b32 v52, 0\n\t"
        "v_mov_b32 v53, 0\n\t"
        "v_mov_b32 v54, 0\n\t"
        "v_mov_b32 v55, 0\n\t"
        "v_mov_b32 v56, 0\n\t"
        "v_mov_b32 v57, 0\n\t"
        "v_mov_b32 v58, 0\n\t"
        "v_mov_b32 v59, 0\n\t"
        "v_mov_b32 v60, 0\n\t"
        "v_mov_b32 v61, 0\n\t"
        "v_mov_b32 v62, 0\n\t"
        "v_mov_b32 v63, 0\n\t"
        "s_nop 1\n\t"
        "s_waitcnt lgkmcnt(14)\n\t"
        "v_mfma_f32_16x16x32_bf16 v[60:63], v[64:67], v[124:127], v[60:63]\n\t"
        "s_waitcnt lgkmcnt(13)\n\t"
        "v_mfma_f32_16x16x32_bf16 v[56:59], v[64:67], v[120:123], v[56:59]\n\t"
        "ds_read_b128 v[128:131], %[aa] offset:64\n\t"
        "s_waitcnt lgkmcnt(13)\n\t"
        "v_mfma_f32_16x16x32_bf16 v[52:55], v[64:67], v[116:119], v[52:55]\n\t"
        "ds_read_b128 v[188:191], %[ba] offset:64\n\t"
        "s_waitcnt lgkmcnt(13)\n\t"
        "v_mfma_f32_16x16x32_bf16 v[60:63], v[68:71], v[120:123], v[60:63]\n\t"
        "v_mfma_f32_16x16x32_bf16 v[56:59], v[68:71], v[116:119], v[56:59]\n\t"
        "ds_read_b128 v[184:187], %[ba] offset:576\n\t"
        "s_waitcnt lgkmcnt(13)\n\t"
        "v_mfma_f32_16x16x32_bf16 v[48:51], v[64:67], v[112:115], v[48:51]\n\t"
        "s_waitcnt lgkmcnt(12)\n\t"
        "v_mfma_f32_16x16x32_bf16 v[60:63], v[72:75], v[116:119], v[60:63]\n\t"
        "ds_read_b128 v[180:183], %[ba] offset:1088\n\t"
        "v_mfma_f32_16x16x32_bf16 v[52:55], v[68:71], v[112:115], v[52:55]\n\t"
        "v_mfma_f32_16x16x32_bf16 v[56:59], v[72:75], v[112:115], v[56:59]\n\t"
        "ds_read_b128 v[132:135], %[aa] offset:576\n\t"
        "s_waitcnt lgkmcnt(13)\n\t"
        "v_mfma_f32_16x16x32_bf16 v[60:63], v[76:79], v[112:115], v[60:63]\n\t"
        "ds_read_b128 v[176:179], %[ba] offset:1600\n\t"
        "s_waitcnt lgkmcnt(13)\n\t"
        "v_mfma_f32_16x16x32_bf16 v[44:47], v[64:67], v[108:111], v[44:47]\n\t"
        "v_mfma_f32_16x16x32_bf16 v[48:51], v[68:71], v[108:111], v[48:51]\n\t"
        "ds_read_b128 v[136:139], %[aa] offset:1088\n\t"
        "v_mfma_f32_16x16x32_bf16 v[52:55], v[72:75], v[108:111], v[52:55]\n\t"
        "v_mfma_f32_16x16x32_bf16 v[56:59], v[76:79], v[108:111], v[56:59]\n\t"
        "ds_read_b128 v[140:143], %[aa] offset:1600\n\t"
        "s_waitcnt lgkmcnt(14)\n\t"
        "v_mfma_f32_16x16x32_bf16 v[60:63], v[80:83], v[108:111], v[60:63]\n\t"
        "s_waitcnt lgkmcnt(13)\n\t"
        "v_mfma_f32_16x16x32_bf16 v[40:43], v[64:67], v[104:107], v[40:43]\n\t"
        "ds_read_b128 v[172:175], %[ba] offset:2112\n\t"
        "v_mfma_f32_16x16x32_bf16 v[44:47], v[68:71], v[104:107], v[44:47]\n\t"
        "ds_read_b128 v[144:147], %[aa] offset:2112\n\t"
        "v_mfma_f32_16x16x32_bf16 v[48:51], v[72:75], v[104:107], v[48:51]\n\t"
        "v_mfma_f32_16x16x32_bf16 v[52:55], v[76:79], v[104:107], v[52:55]\n\t"
        "ds_read_b128 v[168:171], %[ba] offset:2624\n\t"
        "v_mfma_f32_16x16x32_bf16 v[56:59], v[80:83], v[104:107], v[56:59]\n\t"
        "s_waitcnt lgkmcnt(15)\n\t"
        "v_mfma_f32_16x16x32_bf16 v[60:63], v[84:87], v[104:107], v[60:63]\n\t"
        "ds_read_b128 v[148:151], %[aa] offset:2624\n\t"
        "s_waitcnt lgkmcnt(15)\n\t"
        "v_mfma_f32_16x16x32_bf16 v[36:39], v[64:67], v[100:103], v[36:39]\n\t"
        "v_mfma_f32_16x16x32_bf16 v[40:43], v[68:71], v[100:103], v[40:43]\n\t"
        "ds_read_b128 v[164:167], %[ba] offset:3136\n\t"
        "v_mfma_f32_16x16x32_bf16 v[44:47], v[72:75], v[100:103], v[44:47]\n\t"
        "ds_read_b128 v[152:155], %[aa] offset:3136\n\t"
        "v_mfma_f32_16x16x32_bf16 v[48:51], v[76:79], v[100:103], v[48:51]\n\t"
        "v_mfma_f32_16x16x32_bf16 v[52:55], v[80:83], v[100:103], v[52:55]\n\t"
        "ds_read_b128 v[160:163], %[ba] offset:3648\n\t"
        "v_mfma_f32_16x16x32_bf16 v[56:59], v[84:87], v[100:103], v[56:59]\n\t"
        "s_waitcnt lgkmcnt(15)\n\t"
        "v_mfma_f32_16x16x32_bf16 v[60:63], v[88:91], v[100:103], v[60:63]\n\t"
        "ds_read_b128 v[156:159], %[aa] offset:3648\n\t"
        "s_waitcnt lgkmcnt(15)\n\t"
        "v_mfma_f32_16x16x32_bf16 v[32:35], v[64:67], v[96:99], v[32:35]\n\t"
        "v_mfma_f32_16x16x32_bf16 v[36:39], v[68:71], v[96:99], v[36:39]\n\t"
        "v_mfma_f32_16x16x32_bf16 v[40:43], v[72:75], v[96:99], v[40:43]\n\t"
        "v_mfma_f32_16x16x32_bf16 v[44:47], v[76:79], v[96:99], v[44:47]\n\t"
        "v_mfma_f32_16x16x32_bf16 v[48:51], v[80:83], v[96:99], v[48:51]\n\t"
        "v_mfma_f32_16x16x32_bf16 v[52:55], v[84:87], v[96:99], v[52:55]\n\t"
        "v_mfma_f32_16x16x32_bf16 v[56:59], v[88:91], v[96:99], v[56:59]\n\t"
        "s_waitcnt lgkmcnt(15)\n\t"
        "v_mfma_f32_16x16x32_bf16 v[60:63], v[92:95], v[96:99], v[60:63]\n\t"
        "v_mfma_f32_16x16x32_bf16 v[32:35], %[ad], v[96:99], v[32:35]\n\t"
        "v_mfma_f32_16x16x32_bf16 v[36:39], %[ad], v[100:103], v[36:39]\n\t"
        "v_mfma_f32_16x16x32_bf16 v[40:43], %[ad], v[104:107], v[40:43]\n\t"
        "v_mfma_f32_16x16x32_bf16 v[44:47], %[ad], v[108:111], v[44:47]\n\t"
        "v_mfma_f32_16x16x32_bf16 v[48:51], %[ad], v[112:115], v[48:51]\n\t"
        "v_mfma_f32_16x16x32_bf16 v[52:55], %[ad], v[116:119], v[52:55]\n\t"
        "v_mfma_f32_16x16x32_bf16 v[56:59], %[ad], v[120:123], v[56:59]\n\t"
        "v_mfma_f32_16x16x32_bf16 v[60:63], %[ad], v[124:127], v[60:63]\n\t"
        "s_waitcnt lgkmcnt(7)\n\t"
        "v_mfma_f32_16x16x32_bf16 v[60:63], v[128:131], v[188:191], v[60:63]\n\t"
        "v_mfma_f32_16x16x32_bf16 v[56:59], v[128:131], v[184:187], v[56:59]\n\t"
        "ds_read_b128 v[64:67], %[aa] offset:128\n\t"
        "v_mfma_f32_16x16x32_bf16 v[52:55], v[128:131], v[180:183], v[52:55]\n\t"
        "ds_read_b128 v[124:127], %[ba] offset:128\n\t"
        "v_mfma_f32_16x16x32_bf16 v[60:63], v[132:135], v[184:187], v[60:63]\n\t"
        "v_mfma_f32_16x16x32_bf16 v[56:59], v[132:135], v[180:183], v[56:59]\n\t"
        "ds_read_b128 v[120:123], %[ba] offset:640\n\t"
        "v_mfma_f32_16x16x32_bf16 v[48:51], v[128:131], v[176:179], v[48:51]\n\t"
        "v_mfma_f32_16x16x32_bf16 v[60:63], v[136:139], v[180:183], v[60:63]\n\t"
        "ds_read_b128 v[116:119], %[ba] offset:1152\n\t"
        "v_mfma_f32_16x16x32_bf16 v[52:55], v[132:135], v[176:179], v[52:55]\n\t"
        "v_mfma_f32_16x16x32_bf16 v[56:59], v[136:139], v[176:179], v[56:59]\n\t"
        "ds_read_b128 v[68:71], %[aa] offset:640\n\t"
        "v_mfma_f32_16x16x32_bf16 v[60:63], v[140:143], v[176:179], v[60:63]\n\t"
        "ds_read_b128 v[112:115], %[ba] offset:1664\n\t"
        "s_waitcnt lgkmcnt(6)\n\t"
        "v_mfma_f32_16x16x32_bf16 v[44:47], v[128:131], v[172:175], v[44:47]\n\t"
        "v_mfma_f32_16x16x32_bf16 v[48:51], v[132:135], v[172:175], v[48:51]\n\t"
        "ds_read_b128 v[72:75], %[aa] offset:1152\n\t"
        "v_mfma_f32_16x16x32_bf16 v[52:55], v[136:139], v[172:175], v[52:55]\n\t"
        "v_mfma_f32_16x16x32_bf16 v[56:59], v[140:143], v[172:175], v[56:59]\n\t"
        "ds_read_b128 v[76:79], %[aa] offset:1664\n\t"
        "v_mfma_f32_16x16x32_bf16 v[60:63], v[144:147], v[172:175], v[60:63]\n\t"
        "v_mfma_f32_16x16x32_bf16 v[40:43], v[128:131], v[168:171], v[40:43]\n\t"
        "ds_read_b128 v[108:111], %[ba] offset:2176\n\t"
        "v_mfma_f32_16x16x32_bf16 v[44:47], v[132:135], v[168:171], v[44:47]\n\t"
        "ds_read_b128 v[80:83], %[aa] offset:2176\n\t"
        "v_mfma_f32_16x16x32_bf16 v[48:51], v[136:139], v[168:171], v[48:51]\n\t"
        "v_mfma_f32_16x16x32_bf16 v[52:55], v[140:143], v[168:171], v[52:55]\n\t"
        "ds_read_b128 v[104:107], %[ba] offset:2688\n\t"
        "v_mfma_f32_16x16x32_bf16 v[56:59], v[144:147], v[168:171], v[56:59]\n\t"
        "v_mfma_f32_16x16x32_bf16 v[60:63], v[148:151], v[168:171], v[60:63]\n\t"
        "ds_read_b128 v[84:87], %[aa] offset:2688\n\t"
        "v_mfma_f32_16x16x32_bf16 v[36:39], v[128:131], v[164:167], v[36:39]\n\t"
        "v_mfma_f32_16x16x32_bf16 v[40:43], v[132:135], v[164:167], v[40:43]\n\t"
        "ds_read_b128 v[100:103], %[ba] offset:3200\n\t"
        "v_mfma_f32_16x16x32_bf16 v[44:47], v[136:139], v[164:167], v[44:47]\n\t"
        "ds_read_b128 v[88:91], %[aa] offset:3200\n\t"
        "v_mfma_f32_16x16x32_bf16 v[48:51], v[140:143], v[164:167], v[48:51]\n\t"
        "v_mfma_f32_16x16x32_bf16 v[52:55], v[144:147], v[164:167], v[52:55]\n\t"
        "ds_read_b128 v[96:99], %[ba] offset:3712\n\t"
        "v_mfma_f32_16x16x32_bf16 v[56:59], v[148:151], v[164:167], v[56:59]\n\t"
        "v_mfma_f32_16x16x32_bf16 v[60:63], v[152:155], v[164:167], v[60:63]\n\t"
        "ds_read_b128 v[92:95], %[aa] offset:3712\n\t"
        "v_mfma_f32_16x16x32_bf16 v[32:35], v[128:131], v[160:163], v[32:35]\n\t"
        "v_mfma_f32_16x16x32_bf16 v[36:39], v[132:135], v[160:163], v[36:39]\n\t"
        "v_mfma_f32_16x16x32_bf16 v[40:43], v[136:139], v[160:163], v[40:43]\n\t"
        "v_mfma_f32_16x16x32_bf16 v[44:47], v[140:143], v[160:163], v[44:47]\n\t"
        "v_mfma_f32_16x16x32_bf16 v[48:51], v[144:147], v[160:163], v[48:51]\n\t"
        "v_mfma_f32_16x16x32_bf16 v[52:55], v[148:151], v[160:163], v[52:55]\n\t"
        "v_mfma_f32_16x16x32_bf16 v[56:59], v[152:155], v[160:163], v[56:59]\n\t"
        "v_mfma_f32_16x16x32_bf16 v[60:63], v[156:159], v[160:163], v[60:63]\n\t"
        "s_waitcnt lgkmcnt(7)\n\t"
        "v_mfma_f32_16x16x32_bf16 v[60:63], v[64:67], v[124:127], v[60:63]\n\t"
        "v_mfma_f32_16x16x32_bf16 v[56:59], v[64:67], v[120:123], v[56:59]\n\t"
        "ds_read_b128 v[128:131], %[aa] offset:192\n\t"
        "v_mfma_f32_16x16x32_bf16 v[52:55], v[64:67], v[116:119], v[52:55]\n\t"
        "ds_read_b128 v[188:191], %[ba] offset:192\n\t"
        "v_mfma_f32_16x16x32_bf16 v[60:63], v[68:71], v[120:123], v[60:63]\n\t"
        "v_mfma_f32_16x16x32_bf16 v[56:59], v[68:71], v[116:119], v[56:59]\n\t"
        "ds_read_b128 v[184:187], %[ba] offset:704\n\t"
        "v_mfma_f32_16x16x32_bf16 v[48:51], v[64:67], v[112:115], v[48:51]\n\t"
        "v_mfma_f32_16x16x32_bf16 v[60:63], v[72:75], v[116:119], v[60:63]\n\t"
        "ds_read_b128 v[180:183], %[ba] offset:1216\n\t"
        "v_mfma_f32_16x16x32_bf16 v[52:55], v[68:71], v[112:115], v[52:55]\n\t"
        "v_mfma_f32_16x16x32_bf16 v[56:59], v[72:75], v[112:115], v[56:59]\n\t"
        "ds_read_b128 v[132:135], %[aa] offset:704\n\t"
        "v_mfma_f32_16x16x32_bf16 v[60:63], v[76:79], v[112:115], v[60:63]\n\t"
        "ds_read_b128 v[176:179], %[ba] offset:1728\n\t"
        "s_waitcnt lgkmcnt(6)\n\t"
        "v_mfma_f32_16x16x32_bf16 v[44:47], v[64:67], v[108:111], v[44:47]\n\t"
        "v_mfma_f32_16x16x32_bf16 v[48:51], v[68:71], v[108:111], v[48:51]\n\t"
        "ds_read_b128 v[136:139], %[aa] offset:1216\n\t"
        "v_mfma_f32_16x16x32_bf16 v[52:55], v[72:75], v[108:111], v[52:55]\n\t"
        "v_mfma_f32_16x16x32_bf16 v[56:59], v[76:79], v[108:111], v[56:59]\n\t"
        "ds_read_b128 v[140:143], %[aa] offset:1728\n\t"
        "v_mfma_f32_16x16x32_bf16 v[60:63], v[80:83], v[108:111], v[60:63]\n\t"
        "v_mfma_f32_16x16x32_bf16 v[40:43], v[64:67], v[104:107], v[40:43]\n\t"
        "ds_read_b128 v[172:175], %[ba] offset:2240\n\t"
        "v_mfma_f32_16x16x32_bf16 v[44:47], v[68:71], v[104:107], v[44:47]\n\t"
        "ds_read_b128 v[144:147], %[aa] offset:2240\n\t"
        "v_mfma_f32_16x16x32_bf16 v[48:51], v[72:75], v[104:107], v[48:51]\n\t"
        "v_mfma_f32_16x16x32_bf16 v[52:55], v[76:79], v[104:107], v[52:55]\n\t"
        "ds_read_b128 v[168:171], %[ba] offset:2752\n\t"
        "v_mfma_f32_16x16x32_bf16 v[56:59], v[80:83], v[104:107], v[56:59]\n\t"
        "v_mfma_f32_16x16x32_bf16 v[60:63], v[84:87], v[104:107], v[60:63]\n\t"
        "ds_read_b128 v[148:151], %[aa] offset:2752\n\t"
        "v_mfma_f32_16x16x32_bf16 v[36:39], v[64:67], v[100:103], v[36:39]\n\t"
        "v_mfma_f32_16x16x32_bf16 v[40:43], v[68:71], v[100:103], v[40:43]\n\t"
        "ds_read_b128 v[164:167], %[ba] offset:3264\n\t"
        "v_mfma_f32_16x16x32_bf16 v[44:47], v[72:75], v[100:103], v[44:47]\n\t"
        "ds_read_b128 v[152:155], %[aa] offset:3264\n\t"
        "v_mfma_f32_16x16x32_bf16 v[48:51], v[76:79], v[100:103], v[48:51]\n\t"
        "v_mfma_f32_16x16x32_bf16 v[52:55], v[80:83], v[100:103], v[52:55]\n\t"
        "ds_read_b128 v[160:163], %[ba] offset:3776\n\t"
        "v_mfma_f32_16x16x32_bf16 v[56:59], v[84:87], v[100:103], v[56:59]\n\t"
        "v_mfma_f32_16x16x32_bf16 v[60:63], v[88:91], v[100:103], v[60:63]\n\t"
        "ds_read_b128 v[156:159], %[aa] offset:3776\n\t"
        "v_mfma_f32_16x16x32_bf16 v[32:35], v[64:67], v[96:99], v[32:35]\n\t"
        "v_mfma_f32_16x16x32_bf16 v[36:39], v[68:71], v[96:99], v[36:39]\n\t"
        "v_mfma_f32_16x16x32_bf16 v[40:43], v[72:75], v[96:99], v[40:43]\n\t"
        "v_mfma_f32_16x16x32_bf16 v[44:47], v[76:79], v[96:99], v[44:47]\n\t"
        "v_mfma_f32_16x16x32_bf16 v[48:51], v[80:83], v[96:99], v[48:51]\n\t"
        "v_mfma_f32_16x16x32_bf16 v[52:55], v[84:87], v[96:99], v[52:55]\n\t"
        "v_mfma_f32_16x16x32_bf16 v[56:59], v[88:91], v[96:99], v[56:59]\n\t"
        "v_mfma_f32_16x16x32_bf16 v[60:63], v[92:95], v[96:99], v[60:63]\n\t"
        "s_waitcnt lgkmcnt(7)\n\t"
        "v_mfma_f32_16x16x32_bf16 v[60:63], v[128:131], v[188:191], v[60:63]\n\t"
        "v_mfma_f32_16x16x32_bf16 v[56:59], v[128:131], v[184:187], v[56:59]\n\t"
        "v_mfma_f32_16x16x32_bf16 v[52:55], v[128:131], v[180:183], v[52:55]\n\t"
        "v_mfma_f32_16x16x32_bf16 v[60:63], v[132:135], v[184:187], v[60:63]\n\t"
        "v_mfma_f32_16x16x32_bf16 v[56:59], v[132:135], v[180:183], v[56:59]\n\t"
        "v_mfma_f32_16x16x32_bf16 v[48:51], v[128:131], v[176:179], v[48:51]\n\t"
        "v_mfma_f32_16x16x32_bf16 v[60:63], v[136:139], v[180:183], v[60:63]\n\t"
        "v_mfma_f32_16x16x32_bf16 v[52:55], v[132:135], v[176:179], v[52:55]\n\t"
        "v_mfma_f32_16x16x32_bf16 v[56:59], v[136:139], v[176:179], v[56:59]\n\t"
        "v_mfma_f32_16x16x32_bf16 v[60:63], v[140:143], v[176:179], v[60:63]\n\t"
        "s_waitcnt lgkmcnt(0)\n\t"
        "v_mfma_f32_16x16x32_bf16 v[44:47], v[128:131], v[172:175], v[44:47]\n\t"
        "v_mfma_f32_16x16x32_bf16 v[48:51], v[132:135], v[172:175], v[48:51]\n\t"
        "v_mfma_f32_16x16x32_bf16 v[52:55], v[136:139], v[172:175], v[52:55]\n\t"
        "v_mfma_f32_16x16x32_bf16 v[56:59], v[140:143], v[172:175], v[56:59]\n\t"
        "v_mfma_f32_16x16x32_bf16 v[60:63], v[144:147], v[172:175], v[60:63]\n\t"
        "v_mfma_f32_16x16x32_bf16 v[40:43], v[128:131], v[168:171], v[40:43]\n\t"
        "v_mfma_f32_16x16x32_bf16 v[44:47], v[132:135], v[168:171], v[44:47]\n\t"
        "v_mfma_f32_16x16x32_bf16 v[48:51], v[136:139], v[168:171], v[48:51]\n\t"
        "v_mfma_f32_16x16x32_bf16 v[52:55], v[140:143], v[168:171], v[52:55]\n\t"
        "v_mfma_f32_16x16x32_bf16 v[56:59], v[144:147], v[168:171], v[56:59]\n\t"
        "v_mfma_f32_16x16x32_bf16 v[60:63], v[148:151], v[168:171], v[60:63]\n\t"
        "v_mfma_f32_16x16x32_bf16 v[36:39], v[128:131], v[164:167], v[36:39]\n\t"
        "v_mfma_f32_16x16x32_bf16 v[40:43], v[132:135], v[164:167], v[40:43]\n\t"
        "v_mfma_f32_16x16x32_bf16 v[44:47], v[136:139], v[164:167], v[44:47]\n\t"
        "v_mfma_f32_16x16x32_bf16 v[48:51], v[140:143], v[164:167], v[48:51]\n\t"
        "v_mfma_f32_16x16x32_bf16 v[52:55], v[144:147], v[164:167], v[52:55]\n\t"
        "v_mfma_f32_16x16x32_bf16 v[56:59], v[148:151], v[164:167], v[56:59]\n\t"
        "v_mfma_f32_16x16x32_bf16 v[60:63], v[152:155], v[164:167], v[60:63]\n\t"
        "v_mfma_f32_16x16x32_bf16 v[32:35], v[128:131], v[160:163], v[32:35]\n\t"
        "v_mfma_f32_16x16x32_bf16 v[36:39], v[132:135], v[160:163], v[36:39]\n\t"
        "v_mfma_f32_16x16x32_bf16 v[40:43], v[136:139], v[160:163], v[40:43]\n\t"
        "v_mfma_f32_16x16x32_bf16 v[44:47], v[140:143], v[160:163], v[44:47]\n\t"
        "v_mfma_f32_16x16x32_bf16 v[48:51], v[144:147], v[160:163], v[48:51]\n\t"
        "v_mfma_f32_16x16x32_bf16 v[52:55], v[148:151], v[160:163], v[52:55]\n\t"
        "v_mfma_f32_16x16x32_bf16 v[56:59], v[152:155], v[160:163], v[56:59]\n\t"
        "v_mfma_f32_16x16x32_bf16 v[60:63], v[156:159], v[160:163], v[60:63]\n\t"
        "s_nop 15\n\t"
            : "=&{v[32:47]}"(lo), "=&{v[48:63]}"(hi)
            : [aa] "v"(caa), [ba] "v"(cba - 7 * 512), [ad] "v"(cad)
            :
          "v64", "v65", "v66", "v67", "v68", "v69", "v70", "v71", "v72", "v73", "v74", "v75", "v76", "v77", "v78", "v79", "v80", "v81", "v82", "v83", "v84", "v85", "v86", "v87", "v88", "v89", "v90", "v91", "v92", "v93", "v94", "v95", "v96", "v97", "v98", "v99", "v100", "v101", "v102", "v103", "v104", "v105", "v106", "v107", "v108", "v109", "v110", "v111", "v112", "v113", "v114", "v115", "v116", "v117", "v118", "v119", "v120", "v121", "v122", "v123", "v124", "v125", "v126", "v127", "v128", "v129", "v130", "v131", "v132", "v133", "v134", "v135", "v136", "v137", "v138", "v139", "v140", "v141", "v142", "v143", "v144", "v145", "v146", "v147", "v148", "v149", "v150", "v151", "v152", "v153", "v154", "v155", "v156", "v157", "v158", "v159", "v160", "v161", "v162", "v163", "v164", "v165", "v166", "v167", "v168", "v169", "v170", "v171", "v172", "v173", "v174", "v175", "v176", "v177", "v178", "v179", "v180", "v181", "v182", "v183", "v184", "v185", "v186", "v187", "v188", "v189", "v190", "v191", "memory"
        );
#pragma unroll
        for (int n = 0; n < 4; ++n) {
            acc[n] = f32x4{lo[4 * n], lo[4 * n + 1], lo[4 * n + 2], lo[4 * n + 3]};
            acc[4 + n] = f32x4{hi[4 * n], hi[4 * n + 1], hi[4 * n + 2], hi[4 * n + 3]};
        }
        const int hf = w & 1;
#pragma unroll
        for (int n = 0; n < 8; ++n) if ((n >> 2) != hf) lds_st<f32x4>(red + (w * 4 + (n & 3)) * 1024 + lane * 16, acc[n]);
    } else {
#pragma unroll
        for (int n = 0; n < 8; ++n) acc[n] = f32x4{0.f, 0.f, 0.f, 0.f};
        __builtin_amdgcn_s_setprio(2);
        conv_quarter(caa, cba, j4, cad, acc[0], acc[1]);
        __builtin_amdgcn_s_setprio(0);
        if (w > 4) {
            lds_st<f32x4>(red + (16 + 2 * (w - 5)) * 1024 + lane * 16, acc[0]);
            lds_st<f32x4>(red + (17 + 2 * (w - 5)) * 1024 + lane * 16, acc[1]);
        }
        const int hp = h >> 1;
        const int cgx = lane < 16 ? (16 + hp) / 3 : (lane < 32 ? (32 + hp) / 3 : hp / 3);
        const int tbx = lane < 32 ? (lane & 15) : 2 * qb + (lane & 1);
        wait_flags(fb, FL_F1 + (cgx * 16 + tbx) * 8, tag);
        const int u = w - 4;
        const char* kv = p.ws + WS_KV + (size_t)h * KVIMG;
        dma16x4<true, false>(kv + u * 4096, lane * 16, lds + u * 4096);
        dma16x4<true, false>(kv + (u + 4) * 4096, lane * 16, lds + (u + 4) * 4096);
        if (u == 0) dma16x4<true, false>(kv + 8 * 4096, lane * 16, lds + 8 * 4096);
        else if (u == 1) {
            dma16<true>(kv + 36 * 1024, lane * 16, lds + 36 * 1024);
            dma16<true>(kv + 37 * 1024, lane * 16, lds + 37 * 1024);
            dma16<true>(p.ws + WS_QL + ((size_t)h * SEQ + qb * 256) * 4, lane * 16, lds + QST + 8192);
        } else dma16x4<true, false>(p.ws + WS_QM + ((size_t)h * 64 + qb * 8 + (u - 2) * 4) * 1024, lane * 16, lds + QST + (u - 2) * 4096);
        asm volatile("" : "+v"(xa), "+v"(ga), "+v"(xb), "+v"(gb), "+v"(xc), "+v"(xd));
        gb &= (lane < 8 ? 0xffffffffu : 0u);
        float s0 = 0.f, s1 = 0.f;
#pragma unroll
        for (int i = 0; i < 4; ++i) { s0 = dot2(xb[i], gb[i], dot2(xa[i], ga[i], s0)); s1 = dot2(xd[i], gb[i], dot2(xc[i], ga[i], s1)); }
        s0 = wave_sum63(s0); s1 = wave_sum63(s1);
        const float t0s = __uint_as_float(__builtin_amdgcn_readlane(__float_as_uint(s0), 63));
        const float sg = lane == 62 ? t0s : s1;
        st4(ws, lane >= 62 ? WS_G + (tg + lane - 62) * 4 : OOB, __float_as_uint(1.f / (1.f + __expf(-sg))));
    }
    BAR();
    if (w < 4) {
        const int hf = w & 1;
#pragma unroll
        for (int n = 0; n < 8; n += 2)
            if ((n >> 2) == hf)
                conv_store2(ws, ch, n, n + 1, lane, acc[n] + lds_ld<f32x4>(red + ((w ^ 1) * 4 + (n & 3)) * 1024 + lane * 16),
                            acc[n + 1] + lds_ld<f32x4>(red + ((w ^ 1) * 4 + ((n + 1) & 3)) * 1024 + lane * 16));
    } else if (w == 4) {
        f32x4 aH = acc[0], aL = acc[1];
#pragma unroll
        for (int o = 0; o < 3; ++o) {
            aH += lds_ld<f32x4>(red + (16 + 2 * o) * 1024 + lane * 16);
            aL += lds_ld<f32x4>(red + (17 + 2 * o) * 1024 + lane * 16);
        }
        conv_store2(ws, ch, 7 - j4, j4, lane, aH, aL);
    }
}

__device__ __forceinline__ f32x16 qk_tile(rsrc_t ws, u32 oK, u32 oL, int t, bf16x8 qm, bf16x4 ql0, bf16x4 ql1, f32x16 c) {
    const u32 cb = (t >> 2) * CH;
    const u32x2 kl = __builtin_amdgcn_raw_buffer_load_b64(ws, oL + cb + 4096 + ((t & 3) >> 1) * 256, 0, SC1);
    const u32x4 km = ld16(ws, oK + cb + (t & 3) * 1024);
    typedef short s4 __attribute__((ext_vector_type(4)));
    c = __builtin_amdgcn_mfma_f32_32x32x8bf16_1k(__builtin_bit_cast(s4, kl), __builtin_bit_cast(s4, (t & 1) ? ql1 : ql0), c, 0, 0, 0);
    return __builtin_amdgcn_mfma_f32_32x32x16_bf16(__builtin_bit_cast(bf16x8, km), qm, c, 0, 0, 0);
}

__device__ __forceinline__ f32x16 attn_true_max(rsrc_t ws, u32 oK, u32 oL, u32 oV, u32x4 qmr, u32 qlr) {
    const bf16x8 qm = __builtin_bit_cast(bf16x8, qmr);
    const u32x2 q0 = {qlr, 0}, q1 = {0, qlr};
    const bf16x4 ql0 = __builtin_bit_cast(bf16x4, q0), ql1 = __builtin_bit_cast(bf16x4, q1);
    f32x16 z;
#pragma unroll
    for (int i = 0; i < 16; ++i) z[i] = 0.f;
    float m = -__builtin_inff();
#pragma unroll 1
    for (int t = 0; t < 64; ++t) {
        const f32x16 s = qk_tile(ws, oK, oL, t, qm, ql0, ql1, z);
#pragma unroll
        for (int i = 0; i < 16; ++i) m = fmaxf(m, s[i]);
    }
    m = fmaxf(m, __shfl_xor(m, 32));
    f32x16 o = z;
#pragma unroll 1
    for (int t = 0; t < 64; ++t) {
        const f32x16 s = qk_tile(ws, oK, oL, t, qm, ql0, ql1, z);
        const u32 vb = oV + (t >> 2) * CH + 4608 + (t & 3) * (2 * VB);
#pragma unroll
        for (int hf = 0; hf < 2; ++hf) {
            u32x8 pr;
#pragma unroll
            for (int i = 0; i < 8; ++i) pr[i] = __float_as_uint(__builtin_amdgcn_exp2f(s[8 * hf + i] - m));
            o = __builtin_amdgcn_smfmac_f32_32x32x32_bf16(__builtin_bit_cast(bf16x8, ld16(ws, vb + hf * VB)), __builtin_bit_cast(bf16x16, pr), o,
                                                          (int)0xDDDDDDDD, 0, 0);
        }
    }
    return o;
}

__device__ __forceinline__ void phase_attn(const Ptrs& p, rsrc_t ws, int b, u32 lds, u64 tag, const AttnSetup& as) {
    const int tid = threadIdx.x, lane = tid & 63;
    const int w = uni(tid >> 6);
    const int xcd = b & 7, ii = b >> 3;
    const int h = xcd * 4 + (ii >> 3), qb = ii & 7, ng = ii & 7;
    const char* kvp = uni64(p.ws + WS_KV + (size_t)h * KVIMG + w * 5120);
    const u32 m0base = uni(lds + w * 5120);
    u32 xm0[2];
#pragma unroll
    for (int s = 0; s < 2; ++s) {
        const int e = (s == 1 && w < 2) ? 8 + w : w, G = 16 + (e >= 5), j = e - 5 * (e >= 5);
        xm0[s] = uni(lds + G * 5120 + j * 1024);
    }
    const char* wob = uni64((const char*)p.wo + ng * 144);
    const char* wbase = uni64((const char*)p.wo + ng * 144 + (size_t)w * 32 * 1152);
    f32x16 o;
    u32x4 qm; u32 ql;
    if (w == 4) WAITV(2); else if (w > 4) WAITV(1);
    BAR();
    asm volatile(
        "s_mov_b32 s21, m0\n\t"
        "s_cmp_lt_u32 %[wv], 4\n\t"
        "s_cbranch_scc1 3f\n\t"
        "s_setprio 1\n\t"
        "3:\n\t"
        "ds_read_b128 v[172:175], %[qa]\n\t"
        "ds_read_b32 v176, %[qla]\n\t"
        "v_mov_b32 v181, %[aK]\n\t"
        "v_mov_b32 v182, %[aL]\n\t"
        "v_mov_b32 v183, %[aV]\n\t"
        "ds_read_b64 v[168:169], v182 offset:4096\n\t"
        "ds_read_b128 v[144:147], v181 offset:0\n\t"
        "ds_read_b64 v[170:171], v182 offset:4352\n\t"
        "ds_read_b128 v[148:151], v181 offset:1024\n\t"
        "ds_read_b128 v[152:155], v183 offset:4608\n\t"
        "s_mov_b64 s[22:23], %[kvp]\n\t"
        "s_add_u32 s22, s22, 40960\n\t"
        "s_addc_u32 s23, s23, 0\n\t"
        "s_add_u32 s24, %[m0b], 40960\n\t"
        "v_mov_b32 v184, %[voff]\n\t"
        "v_mov_b32 v180, 0xdddddddd\n\t"
        "v_mov_b32 v128, 0\n\t"
        "v_mov_b32 v129, 0\n\t"
        "v_mov_b32 v130, 0\n\t"
        "v_mov_b32 v131, 0\n\t"
        "v_mov_b32 v132, 0\n\t"
        "v_mov_b32 v133, 0\n\t"
        "v_mov_b32 v134, 0\n\t"
        "v_mov_b32 v135, 0\n\t"
        "v_mov_b32 v136, 0\n\t"
        "v_mov_b32 v137, 0\n\t"
        "v_mov_b32 v138, 0\n\t"
        "v_mov_b32 v139, 0\n\t"
        "v_mov_b32 v140, 0\n\t"
        "v_mov_b32 v141, 0\n\t"
        "v_mov_b32 v142, 0\n\t"
        "v_mov_b32 v143, 0\n\t"
        "v_mov_b32 v120, 0\n\t"
        "v_mov_b32 v121, 0\n\t"
        "v_mov_b32 v122, 0\n\t"
        "v_mov_b32 v123, 0\n\t"
        "v_mov_b32 v124, 0\n\t"
        "v_mov_b32 v125, 0\n\t"
        "v_mov_b32 v126, 0\n\t"
        "v_mov_b32 v127, 0\n\t"
        "v_mov_b32 v164, 0\n\t"
        "v_mov_b32 v165, 0\n\t"
        "v_mov_b32 v166, 0\n\t"
        "v_mov_b32 v167, 0\n\t"
        "v_mov_b32 v177, 0\n\t"
        "v_mov_b32 v178, 0\n\t"
        "s_waitcnt lgkmcnt(5)\n\t"
        "v_and_b32 v176, v176, %[hm]\n\t"
        "v_mov_b32 v179, v176\n\t"
        "s_nop 1\n\t"
        "s_waitcnt lgkmcnt(3)\n\t"
        "v_mfma_f32_32x32x8bf16_1k v[64:79], v[168:169], v[176:177], 0\n\t"
        "v_mfma_f32_32x32x16_bf16 v[64:79], v[144:147], v[172:175], v[64:79]\n\t"
        "ds_read_b128 v[160:163], v183 offset:5248\n\t"
        "ds_read_b128 v[144:147], v181 offset:2048\n\t"
        "ds_read_b128 v[156:159], v183 offset:5888\n\t"
        "s_nop 7\n\t"
        "s_mov_b32 s20, 0\n\t"
        "1:\n\t"
        "s_mov_b32 m0, s24\n\t"
        "s_waitcnt lgkmcnt(3)\n\t"
        "v_mfma_f32_32x32x8bf16_1k v[80:95], v[168:169], v[178:179], 0\n\t"
        "ds_read_b64 v[168:169], v182 offset:13824\n\t"
        "global_load_lds_dwordx4 v184, s[22:23] offset:-2048 sc1\n\t"
        "v_exp_f32 v64, v64\n\t"
        "v_exp_f32 v65, v65\n\t"
        "v_exp_f32 v66, v66\n\t"
        "v_exp_f32 v67, v67\n\t"
        "v_smfmac_f32_32x32x32_bf16 v[128:143], v[164:167], v[120:127], v180\n\t"
        "ds_read_b128 v[164:167], v183 offset:6528\n\t"
        "v_exp_f32 v68, v68\n\t"
        "v_exp_f32 v69, v69\n\t"
        "v_exp_f32 v70, v70\n\t"
        "v_exp_f32 v71, v71\n\t"
        "v_mfma_f32_32x32x16_bf16 v[80:95], v[148:151], v[172:175], v[80:95]\n\t"
        "ds_read_b128 v[148:151], v181 offset:3072\n\t"
        "v_exp_f32 v72, v72\n\t"
        "v_exp_f32 v73, v73\n\t"
        "v_exp_f32 v74, v74\n\t"
        "v_exp_f32 v75, v75\n\t"
        "v_smfmac_f32_32x32x32_bf16 v[128:143], v[152:155], v[64:71], v180\n\t"
        "ds_read_b128 v[152:155], v183 offset:7168\n\t"
        "v_exp_f32 v76, v76\n\t"
        "v_exp_f32 v77, v77\n\t"
        "v_exp_f32 v78, v78\n\t"
        "v_exp_f32 v79, v79\n\t"
        "s_waitcnt lgkmcnt(4)\n\t"
        "v_mfma_f32_32x32x8bf16_1k v[96:111], v[170:171], v[176:177], 0\n\t"
        "global_load_lds_dwordx4 v184, s[22:23] offset:-1024 sc1\n\t"
        "v_exp_f32 v80, v80\n\t"
        "v_exp_f32 v81, v81\n\t"
        "v_exp_f32 v82, v82\n\t"
        "v_exp_f32 v83, v83\n\t"
        "v_smfmac_f32_32x32x32_bf16 v[128:143], v[160:163], v[72:79], v180\n\t"
        "ds_read_b128 v[160:163], v183 offset:7808\n\t"
        "v_exp_f32 v84, v84\n\t"
        "v_exp_f32 v85, v85\n\t"
        "v_exp_f32 v86, v86\n\t"
        "v_exp_f32 v87, v87\n\t"
        "v_mfma_f32_32x32x16_bf16 v[96:111], v[144:147], v[172:175], v[96:111]\n\t"
        "ds_read_b128 v[144:147], v181 offset:9728\n\t"
        "v_exp_f32 v88, v88\n\t"
        "v_exp_f32 v89, v89\n\t"
        "v_exp_f32 v90, v90\n\t"
        "v_exp_f32 v91, v91\n\t"
        "v_smfmac_f32_32x32x32_bf16 v[128:143], v[156:159], v[80:87], v180\n\t"
        "ds_read_b128 v[156:159], v183 offset:8448\n\t"
        "v_exp_f32 v92, v92\n\t"
        "v_exp_f32 v93, v93\n\t"
        "v_exp_f32 v94, v94\n\t"
        "v_exp_f32 v95, v95\n\t"
        "s_waitcnt lgkmcnt(3)\n\t"
        "v_mfma_f32_32x32x8bf16_1k v[112:127], v[170:171], v[178:179], 0\n\t"
        "ds_read_b64 v[170:171], v182 offset:14080\n\t"
        "global_load_lds_dwordx4 v184, s[22:23] offset:0 sc1\n\t"
        "v_exp_f32 v96, v96\n\t"
        "v_exp_f32 v97, v97\n\t"
        "v_exp_f32 v98, v98\n\t"
        "v_exp_f32 v99, v99\n\t"
        "v_smfmac_f32_32x32x32_bf16 v[128:143], v[164:167], v[88:95], v180\n\t"
        "ds_read_b128 v[164:167], v183 offset:9088\n\t"
        "v_exp_f32 v100, v100\n\t"
        "v_exp_f32 v101, v101\n\t"
        "v_exp_f32 v102, v102\n\t"
        "v_exp_f32 v103, v103\n\t"
        "v_mfma_f32_32x32x16_bf16 v[112:127], v[148:151], v[172:175], v[112:127]\n\t"
        "ds_read_b128 v[148:151], v181 offset:10752\n\t"
        "v_exp_f32 v104, v104\n\t"
        "v_exp_f32 v105, v105\n\t"
        "v_exp_f32 v106, v106\n\t"
        "v_exp_f32 v107, v107\n\t"
        "v_smfmac_f32_32x32x32_bf16 v[128:143], v[152:155], v[96:103], v180\n\t"
        "ds_read_b128 v[152:155], v183 offset:14336\n\t"
        "v_exp_f32 v108, v108\n\t"
        "v_exp_f32 v109, v109\n\t"
        "v_exp_f32 v110, v110\n\t"
        "v_exp_f32 v111, v111\n\t"
        "s_waitcnt lgkmcnt(4)\n\t"
        "v_mfma_f32_32x32x8bf16_1k v[64:79], v[168:169], v[176:177], 0\n\t"
        "global_load_lds_dwordx4 v184, s[22:23] offset:1024 sc1\n\t"
        "v_exp_f32 v112, v112\n\t"
        "v_exp_f32 v113, v113\n\t"
        "v_exp_f32 v114, v114\n\t"
        "v_exp_f32 v115, v115\n\t"
        "v_smfmac_f32_32x32x32_bf16 v[128:143], v[160:163], v[104:111], v180\n\t"
        "ds_read_b128 v[160:163], v183 offset:14976\n\t"
        "v_exp_f32 v116, v116\n\t"
        "v_exp_f32 v117, v117\n\t"
        "v_exp_f32 v118, v118\n\t"
        "v_exp_f32 v119, v119\n\t"
        "v_mfma_f32_32x32x16_bf16 v[64:79], v[144:147], v[172:175], v[64:79]\n\t"
        "ds_read_b128 v[144:147], v181 offset:11776\n\t"
        "v_exp_f32 v120, v120\n\t"
        "v_exp_f32 v121, v121\n\t"
        "v_exp_f32 v122, v122\n\t"
        "v_exp_f32 v123, v123\n\t"
        "v_smfmac_f32_32x32x32_bf16 v[128:143], v[156:159], v[112:119], v180\n\t"
        "ds_read_b128 v[156:159], v183 offset:15616\n\t"
        "v_exp_f32 v124, v124\n\t"
        "v_exp_f32 v125, v125\n\t"
        "v_exp_f32 v126, v126\n\t"
        "v_exp_f32 v127, v127\n\t"
        "s_waitcnt lgkmcnt(3)\n\t"
        "v_mfma_f32_32x32x8bf16_1k v[80:95], v[168:169], v[178:179], 0\n\t"
        "ds_read_b64 v[168:169], v182 offset:23552\n\t"
        "global_load_lds_dwordx4 v184, s[22:23] offset:2048 sc1\n\t"
        "v_exp_f32 v64, v64\n\t"
        "v_exp_f32 v65, v65\n\t"
        "v_exp_f32 v66, v66\n\t"
        "v_exp_f32 v67, v67\n\t"
        "v_smfmac_f32_32x32x32_bf16 v[128:143], v[164:167], v[120:127], v180\n\t"
        "ds_read_b128 v[164:167], v183 offset:16256\n\t"
        "v_exp_f32 v68, v68\n\t"
        "v_exp_f32 v69, v69\n\t"
        "v_exp_f32 v70, v70\n\t"
        "v_exp_f32 v71, v71\n\t"
        "v_mfma_f32_32x32x16_bf16 v[80:95], v[148:151], v[172:175], v[80:95]\n\t"
        "ds_read_b128 v[148:151], v181 offset:12800\n\t"
        "v_exp_f32 v72, v72\n\t"
        "v_exp_f32 v73, v73\n\t"
        "v_exp_f32 v74, v74\n\t"
        "v_exp_f32 v75, v75\n\t"
        "v_smfmac_f32_32x32x32_bf16 v[128:143], v[152:155], v[64:71], v180\n\t"
        "ds_read_b128 v[152:155], v183 offset:16896\n\t"
        "v_exp_f32 v76, v76\n\t"
        "v_exp_f32 v77, v77\n\t"
        "v_exp_f32 v78, v78\n\t"
        "v_exp_f32 v79, v79\n\t"
        "s_waitcnt lgkmcnt(4)\n\t"
        "v_mfma_f32_32x32x8bf16_1k v[96:111], v[170:171], v[176:177], 0\n\t"
        "v_exp_f32 v80, v80\n\t"
        "v_exp_f32 v81, v81\n\t"
        "v_exp_f32 v82, v82\n\t"
        "v_exp_f32 v83, v83\n\t"
        "v_smfmac_f32_32x32x32_bf16 v[128:143], v[160:163], v[72:79], v180\n\t"
        "ds_read_b128 v[160:163], v183 offset:17536\n\t"
        "v_exp_f32 v84, v84\n\t"
        "v_exp_f32 v85, v85\n\t"
        "v_exp_f32 v86, v86\n\t"
        "v_exp_f32 v87, v87\n\t"
        "v_mfma_f32_32x32x16_bf16 v[96:111], v[144:147], v[172:175], v[96:111]\n\t"
        "ds_read_b128 v[144:147], v181 offset:19456\n\t"
        "v_exp_f32 v88, v88\n\t"
        "v_exp_f32 v89, v89\n\t"
        "v_exp_f32 v90, v90\n\t"
        "v_exp_f32 v91, v91\n\t"
        "v_smfmac_f32_32x32x32_bf16 v[128:143], v[156:159], v[80:87], v180\n\t"
        "ds_read_b128 v[156:159], v183 offset:18176\n\t"
        "v_exp_f32 v92, v92\n\t"
        "v_exp_f32 v93, v93\n\t"
        "v_exp_f32 v94, v94\n\t"
        "v_exp_f32 v95, v95\n\t"
        "s_waitcnt lgkmcnt(3)\n\t"
        "v_mfma_f32_32x32x8bf16_1k v[112:127], v[170:171], v[178:179], 0\n\t"
        "ds_read_b64 v[170:171], v182 offset:23808\n\t"
        "v_exp_f32 v96, v96\n\t"
        "v_exp_f32 v97, v97\n\t"
        "v_exp_f32 v98, v98\n\t"
        "v_exp_f32 v99, v99\n\t"
        "v_smfmac_f32_32x32x32_bf16 v[128:143], v[164:167], v[88:95], v180\n\t"
        "ds_read_b128 v[164:167], v183 offset:18816\n\t"
        "v_exp_f32 v100, v100\n\t"
        "v_exp_f32 v101, v101\n\t"
        "v_exp_f32 v102, v102\n\t"
        "v_exp_f32 v103, v103\n\t"
        "v_mfma_f32_32x32x16_bf16 v[112:127], v[148:151], v[172:175], v[112:127]\n\t"
        "ds_read_b128 v[148:151], v181 offset:20480\n\t"
        "v_exp_f32 v104, v104\n\t"
        "v_exp_f32 v105, v105\n\t"
        "v_exp_f32 v106, v106\n\t"
        "v_exp_f32 v107, v107\n\t"
        "v_smfmac_f32_32x32x32_bf16 v[128:143], v[152:155], v[96:103], v180\n\t"
        "ds_read_b128 v[152:155], v183 offset:24064\n\t"
        "v_exp_f32 v108, v108\n\t"
        "v_exp_f32 v109, v109\n\t"
        "v_exp_f32 v110, v110\n\t"
        "v_exp_f32 v111, v111\n\t"
        "s_waitcnt lgkmcnt(4)\n\t"
        "v_mfma_f32_32x32x8bf16_1k v[64:79], v[168:169], v[176:177], 0\n\t"
        "v_exp_f32 v112, v112\n\t"
        "v_exp_f32 v113, v113\n\t"
        "v_exp_f32 v114, v114\n\t"
        "v_exp_f32 v115, v115\n\t"
        "v_smfmac_f32_32x32x32_bf16 v[128:143], v[160:163], v[104:111], v180\n\t"
        "ds_read_b128 v[160:163], v183 offset:24704\n\t"
        "v_exp_f32 v116, v116\n\t"
        "v_exp_f32 v117, v117\n\t"
        "v_exp_f32 v118, v118\n\t"
        "v_exp_f32 v119, v119\n\t"
        "v_mfma_f32_32x32x16_bf16 v[64:79], v[144:147], v[172:175], v[64:79]\n\t"
        "ds_read_b128 v[144:147], v181 offset:21504\n\t"
        "v_exp_f32 v120, v120\n\t"
        "v_exp_f32 v121, v121\n\t"
        "v_exp_f32 v122, v122\n\t"
        "v_exp_f32 v123, v123\n\t"
        "v_smfmac_f32_32x32x32_bf16 v[128:143], v[156:159], v[112:119], v180\n\t"
        "ds_read_b128 v[156:159], v183 offset:25344\n\t"
        "v_exp_f32 v124, v124\n\t"
        "v_exp_f32 v125, v125\n\t"
        "v_exp_f32 v126, v126\n\t"
        "v_exp_f32 v127, v127\n\t"
        "s_waitcnt lgkmcnt(3)\n\t"
        "v_mfma_f32_32x32x8bf16_1k v[80:95], v[168:169], v[178:179], 0\n\t"
        "ds_read_b64 v[168:169], v182 offset:33280\n\t"
        "v_exp_f32 v64, v64\n\t"
        "v_exp_f32 v65, v65\n\t"
        "v_exp_f32 v66, v66\n\t"
        "v_exp_f32 v67, v67\n\t"
        "v_smfmac_f32_32x32x32_bf16 v[128:143], v[164:167], v[120:127], v180\n\t"
        "ds_read_b128 v[164:167], v183 offset:25984\n\t"
        "v_exp_f32 v68, v68\n\t"
        "v_exp_f32 v69, v69\n\t"
        "v_exp_f32 v70, v70\n\t"
        "v_exp_f32 v71, v71\n\t"
        "v_mfma_f32_32x32x16_bf16 v[80:95], v[148:151], v[172:175], v[80:95]\n\t"
        "ds_read_b128 v[148:151], v181 offset:22528\n\t"
        "v_exp_f32 v72, v72\n\t"
        "v_exp_f32 v73, v73\n\t"
        "v_exp_f32 v74, v74\n\t"
        "v_exp_f32 v75, v75\n\t"
        "v_smfmac_f32_32x32x32_bf16 v[128:143], v[152:155], v[64:71], v180\n\t"
        "ds_read_b128 v[152:155], v183 offset:26624\n\t"
        "v_exp_f32 v76, v76\n\t"
        "v_exp_f32 v77, v77\n\t"
        "v_exp_f32 v78, v78\n\t"
        "v_exp_f32 v79, v79\n\t"
        "s_waitcnt lgkmcnt(4)\n\t"
        "v_mfma_f32_32x32x8bf16_1k v[96:111], v[170:171], v[176:177], 0\n\t"
        "v_exp_f32 v80, v80\n\t"
        "v_exp_f32 v81, v81\n\t"
        "v_exp_f32 v82, v82\n\t"
        "v_exp_f32 v83, v83\n\t"
        "v_smfmac_f32_32x32x32_bf16 v[128:143], v[160:163], v[72:79], v180\n\t"
        "ds_read_b128 v[160:163], v183 offset:27264\n\t"
        "v_exp_f32 v84, v84\n\t"
        "v_exp_f32 v85, v85\n\t"
        "v_exp_f32 v86, v86\n\t"
        "v_exp_f32 v87, v87\n\t"
        "v_mfma_f32_32x32x16_bf16 v[96:111], v[144:147], v[172:175], v[96:111]\n\t"
        "ds_read_b128 v[144:147], v181 offset:29184\n\t"
        "v_exp_f32 v88, v88\n\t"
        "v_exp_f32 v89, v89\n\t"
        "v_exp_f32 v90, v90\n\t"
        "v_exp_f32 v91, v91\n\t"
        "v_smfmac_f32_32x32x32_bf16 v[128:143], v[156:159], v[80:87], v180\n\t"
        "ds_read_b128 v[156:159], v183 offset:27904\n\t"
        "v_exp_f32 v92, v92\n\t"
        "v_exp_f32 v93, v93\n\t"
        "v_exp_f32 v94, v94\n\t"
        "v_exp_f32 v95, v95\n\t"
        "s_waitcnt lgkmcnt(3)\n\t"
        "v_mfma_f32_32x32x8bf16_1k v[112:127], v[170:171], v[178:179], 0\n\t"
        "ds_read_b64 v[170:171], v182 offset:33536\n\t"
        "v_exp_f32 v96, v96\n\t"
        "v_exp_f32 v97, v97\n\t"
        "v_exp_f32 v98, v98\n\t"
        "v_exp_f32 v99, v99\n\t"
        "v_smfmac_f32_32x32x32_bf16 v[128:143], v[164:167], v[88:95], v180\n\t"
        "ds_read_b128 v[164:167], v183 offset:28544\n\t"
        "v_exp_f32 v100, v100\n\t"
        "v_exp_f32 v101, v101\n\t"
        "v_exp_f32 v102, v102\n\t"
        "v_exp_f32 v103, v103\n\t"
        "v_mfma_f32_32x32x16_bf16 v[112:127], v[148:151], v[172:175], v[112:127]\n\t"
        "ds_read_b128 v[148:151], v181 offset:30208\n\t"
        "v_exp_f32 v104, v104\n\t"
        "v_exp_f32 v105, v105\n\t"
        "v_exp_f32 v106, v106\n\t"
        "v_exp_f32 v107, v107\n\t"
        "v_smfmac_f32_32x32x32_bf16 v[128:143], v[152:155], v[96:103], v180\n\t"
        "ds_read_b128 v[152:155], v183 offset:33792\n\t"
        "v_exp_f32 v108, v108\n\t"
        "v_exp_f32 v109, v109\n\t"
        "v_exp_f32 v110, v110\n\t"
        "v_exp_f32 v111, v111\n\t"
        "s_waitcnt lgkmcnt(4)\n\t"
        "v_mfma_f32_32x32x8bf16_1k v[64:79], v[168:169], v[176:177], 0\n\t"
        "v_exp_f32 v112, v112\n\t"
        "v_exp_f32 v113, v113\n\t"
        "v_exp_f32 v114, v114\n\t"
        "v_exp_f32 v115, v115\n\t"
        "v_smfmac_f32_32x32x32_bf16 v[128:143], v[160:163], v[104:111], v180\n\t"
        "ds_read_b128 v[160:163], v183 offset:34432\n\t"
        "v_exp_f32 v116, v116\n\t"
        "v_exp_f32 v117, v117\n\t"
        "v_exp_f32 v118, v118\n\t"
        "v_exp_f32 v119, v119\n\t"
        "v_mfma_f32_32x32x16_bf16 v[64:79], v[144:147], v[172:175], v[64:79]\n\t"
        "ds_read_b128 v[144:147], v181 offset:31232\n\t"
        "v_exp_f32 v120, v120\n\t"
        "v_exp_f32 v121, v121\n\t"
        "v_exp_f32 v122, v122\n\t"
        "v_exp_f32 v123, v123\n\t"
        "v_smfmac_f32_32x32x32_bf16 v[128:143], v[156:159], v[112:119], v180\n\t"
        "ds_read_b128 v[156:159], v183 offset:35072\n\t"
        "v_exp_f32 v124, v124\n\t"
        "v_exp_f32 v125, v125\n\t"
        "v_exp_f32 v126, v126\n\t"
        "v_exp_f32 v127, v127\n\t"
        "s_waitcnt vmcnt(0)\n\t"
        "s_barrier\n\t"
        "s_waitcnt lgkmcnt(3)\n\t"
        "v_mfma_f32_32x32x8bf16_1k v[80:95], v[168:169], v[178:179], 0\n\t"
        "ds_read_b64 v[168:169], v182 offset:43008\n\t"
        "v_exp_f32 v64, v64\n\t"
        "v_exp_f32 v65, v65\n\t"
        "v_exp_f32 v66, v66\n\t"
        "v_exp_f32 v67, v67\n\t"
        "v_smfmac_f32_32x32x32_bf16 v[128:143], v[164:167], v[120:127], v180\n\t"
        "ds_read_b128 v[164:167], v183 offset:35712\n\t"
        "v_exp_f32 v68, v68\n\t"
        "v_exp_f32 v69, v69\n\t"
        "v_exp_f32 v70, v70\n\t"
        "v_exp_f32 v71, v71\n\t"
        "v_mfma_f32_32x32x16_bf16 v[80:95], v[148:151], v[172:175], v[80:95]\n\t"
        "ds_read_b128 v[148:151], v181 offset:32256\n\t"
        "v_exp_f32 v72, v72\n\t"
        "v_exp_f32 v73, v73\n\t"
        "v_exp_f32 v74, v74\n\t"
        "v_exp_f32 v75, v75\n\t"
        "v_smfmac_f32_32x32x32_bf16 v[128:143], v[152:155], v[64:71], v180\n\t"
        "ds_read_b128 v[152:155], v183 offset:36352\n\t"
        "v_exp_f32 v76, v76\n\t"
        "v_exp_f32 v77, v77\n\t"
        "v_exp_f32 v78, v78\n\t"
        "v_exp_f32 v79, v79\n\t"
        "s_waitcnt lgkmcnt(4)\n\t"
        "v_mfma_f32_32x32x8bf16_1k v[96:111], v[170:171], v[176:177], 0\n\t"
        "v_exp_f32 v80, v80\n\t"
        "v_exp_f32 v81, v81\n\t"
        "v_exp_f32 v82, v82\n\t"
        "v_exp_f32 v83, v83\n\t"
        "v_smfmac_f32_32x32x32_bf16 v[128:143], v[160:163], v[72:79], v180\n\t"
        "ds_read_b128 v[160:163], v183 offset:36992\n\t"
        "v_exp_f32 v84, v84\n\t"
        "v_exp_f32 v85, v85\n\t"
        "v_exp_f32 v86, v86\n\t"
        "v_exp_f32 v87, v87\n\t"
        "v_mfma_f32_32x32x16_bf16 v[96:111], v[144:147], v[172:175], v[96:111]\n\t"
        "ds_read_b128 v[144:147], v181 offset:38912\n\t"
        "v_exp_f32 v88, v88\n\t"
        "v_exp_f32 v89, v89\n\t"
        "v_exp_f32 v90, v90\n\t"
        "v_exp_f32 v91, v91\n\t"
        "v_smfmac_f32_32x32x32_bf16 v[128:143], v[156:159], v[80:87], v180\n\t"
        "ds_read_b128 v[156:159], v183 offset:37632\n\t"
        "v_exp_f32 v92, v92\n\t"
        "v_exp_f32 v93, v93\n\t"
        "v_exp_f32 v94, v94\n\t"
        "v_exp_f32 v95, v95\n\t"
        "s_waitcnt lgkmcnt(3)\n\t"
        "v_mfma_f32_32x32x8bf16_1k v[112:127], v[170:171], v[178:179], 0\n\t"
        "ds_read_b64 v[170:171], v182 offset:43264\n\t"
        "v_exp_f32 v96, v96\n\t"
        "v_exp_f32 v97, v97\n\t"
        "v_exp_f32 v98, v98\n\t"
        "v_exp_f32 v99, v99\n\t"
        "v_smfmac_f32_32x32x32_bf16 v[128:143], v[164:167], v[88:95], v180\n\t"
        "ds_read_b128 v[164:167], v183 offset:38272\n\t"
        "v_exp_f32 v100, v100\n\t"
        "v_exp_f32 v101, v101\n\t"
        "v_exp_f32 v102, v102\n\t"
        "v_exp_f32 v103, v103\n\t"
        "v_mfma_f32_32x32x16_bf16 v[112:127], v[148:151], v[172:175], v[112:127]\n\t"
        "ds_read_b128 v[148:151], v181 offset:39936\n\t"
        "v_exp_f32 v104, v104\n\t"
        "v_exp_f32 v105, v105\n\t"
        "v_exp_f32 v106, v106\n\t"
        "v_exp_f32 v107, v107\n\t"
        "v_smfmac_f32_32x32x32_bf16 v[128:143], v[152:155], v[96:103], v180\n\t"
        "ds_read_b128 v[152:155], v183 offset:43520\n\t"
        "v_exp_f32 v108, v108\n\t"
        "v_exp_f32 v109, v109\n\t"
        "v_exp_f32 v110, v110\n\t"
        "v_exp_f32 v111, v111\n\t"
        "s_waitcnt lgkmcnt(4)\n\t"
        "v_mfma_f32_32x32x8bf16_1k v[64:79], v[168:169], v[176:177], 0\n\t"
        "v_exp_f32 v112, v112\n\t"
        "v_exp_f32 v113, v113\n\t"
        "v_exp_f32 v114, v114\n\t"
        "v_exp_f32 v115, v115\n\t"
        "v_smfmac_f32_32x32x32_bf16 v[128:143], v[160:163], v[104:111], v180\n\t"
        "ds_read_b128 v[160:163], v183 offset:44160\n\t"
        "v_exp_f32 v116, v116\n\t"
        "v_exp_f32 v117, v117\n\t"
        "v_exp_f32 v118, v118\n\t"
        "v_exp_f32 v119, v119\n\t"
        "v_mfma_f32_32x32x16_bf16 v[64:79], v[144:147], v[172:175], v[64:79]\n\t"
        "ds_read_b128 v[144:147], v181 offset:40960\n\t"
        "v_exp_f32 v120, v120\n\t"
        "v_exp_f32 v121, v121\n\t"
        "v_exp_f32 v122, v122\n\t"
        "v_exp_f32 v123, v123\n\t"
        "v_smfmac_f32_32x32x32_bf16 v[128:143], v[156:159], v[112:119], v180\n\t"
        "ds_read_b128 v[156:159], v183 offset:44800\n\t"
        "v_exp_f32 v124, v124\n\t"
        "v_exp_f32 v125, v125\n\t"
        "v_exp_f32 v126, v126\n\t"
        "v_exp_f32 v127, v127\n\t"
        "v_add_u32 v181, 38912, v181\n\t"
        "v_add_u32 v182, 38912, v182\n\t"
        "v_add_u32 v183, 38912, v183\n\t"
        "s_add_u32 s22, s22, 38912\n\t"
        "s_addc_u32 s23, s23, 0\n\t"
        "s_add_u32 s24, s24, 38912\n\t"
        "s_add_u32 s20, s20, 1\n\t"
        "s_cmp_lg_u32 s20, 3\n\t"
        "s_cbranch_scc1 1b\n\t"
        "s_mov_b64 s[22:23], %[wbase]\n\t"
        "s_mov_b32 m0, %[wm0]\n\t"
        "s_waitcnt lgkmcnt(3)\n\t"
        "v_mfma_f32_32x32x8bf16_1k v[80:95], v[168:169], v[178:179], 0\n\t"
        "ds_read_b64 v[168:169], v182 offset:13824\n\t"
        "global_load_lds_dwordx4 %[wf0], s[22:23] offset:-2048\n\t"
        "v_exp_f32 v64, v64\n\t"
        "v_exp_f32 v65, v65\n\t"
        "v_exp_f32 v66, v66\n\t"
        "v_exp_f32 v67, v67\n\t"
        "v_smfmac_f32_32x32x32_bf16 v[128:143], v[164:167], v[120:127], v180\n\t"
        "ds_read_b128 v[164:167], v183 offset:6528\n\t"
        "v_exp_f32 v68, v68\n\t"
        "v_exp_f32 v69, v69\n\t"
        "v_exp_f32 v70, v70\n\t"
        "v_exp_f32 v71, v71\n\t"
        "v_mfma_f32_32x32x16_bf16 v[80:95], v[148:151], v[172:175], v[80:95]\n\t"
        "ds_read_b128 v[148:151], v181 offset:3072\n\t"
        "v_exp_f32 v72, v72\n\t"
        "v_exp_f32 v73, v73\n\t"
        "v_exp_f32 v74, v74\n\t"
        "v_exp_f32 v75, v75\n\t"
        "v_smfmac_f32_32x32x32_bf16 v[128:143], v[152:155], v[64:71], v180\n\t"
        "ds_read_b128 v[152:155], v183 offset:7168\n\t"
        "v_exp_f32 v76, v76\n\t"
        "v_exp_f32 v77, v77\n\t"
        "v_exp_f32 v78, v78\n\t"
        "v_exp_f32 v79, v79\n\t"
        "s_waitcnt lgkmcnt(4)\n\t"
        "v_mfma_f32_32x32x8bf16_1k v[96:111], v[170:171], v[176:177], 0\n\t"
        "global_load_lds_dwordx4 %[wf1], s[22:23] offset:-1024\n\t"
        "v_exp_f32 v80, v80\n\t"
        "v_exp_f32 v81, v81\n\t"
        "v_exp_f32 v82, v82\n\t"
        "v_exp_f32 v83, v83\n\t"
        "v_smfmac_f32_32x32x32_bf16 v[128:143], v[160:163], v[72:79], v180\n\t"
        "ds_read_b128 v[160:163], v183 offset:7808\n\t"
        "v_exp_f32 v84, v84\n\t"
        "v_exp_f32 v85, v85\n\t"
        "v_exp_f32 v86, v86\n\t"
        "v_exp_f32 v87, v87\n\t"
        "v_mfma_f32_32x32x16_bf16 v[96:111], v[144:147], v[172:175], v[96:111]\n\t"
        "ds_read_b128 v[144:147], v181 offset:9728\n\t"
        "v_exp_f32 v88, v88\n\t"
        "v_exp_f32 v89, v89\n\t"
        "v_exp_f32 v90, v90\n\t"
        "v_exp_f32 v91, v91\n\t"
        "v_smfmac_f32_32x32x32_bf16 v[128:143], v[156:159], v[80:87], v180\n\t"
        "ds_read_b128 v[156:159], v183 offset:8448\n\t"
        "v_exp_f32 v92, v92\n\t"
        "v_exp_f32 v93, v93\n\t"
        "v_exp_f32 v94, v94\n\t"
        "v_exp_f32 v95, v95\n\t"
        "s_barrier\n\t"
        "s_waitcnt lgkmcnt(3)\n\t"
        "v_mfma_f32_32x32x8bf16_1k v[112:127], v[170:171], v[178:179], 0\n\t"
        "ds_read_b64 v[170:171], v182 offset:14080\n\t"
        "global_load_lds_dwordx4 %[wf2], s[22:23] offset:0\n\t"
        "v_exp_f32 v96, v96\n\t"
        "v_exp_f32 v97, v97\n\t"
        "v_exp_f32 v98, v98\n\t"
        "v_exp_f32 v99, v99\n\t"
        "v_smfmac_f32_32x32x32_bf16 v[128:143], v[164:167], v[88:95], v180\n\t"
        "ds_read_b128 v[164:167], v183 offset:9088\n\t"
        "v_exp_f32 v100, v100\n\t"
        "v_exp_f32 v101, v101\n\t"
        "v_exp_f32 v102, v102\n\t"
        "v_exp_f32 v103, v103\n\t"
        "v_mfma_f32_32x32x16_bf16 v[112:127], v[148:151], v[172:175], v[112:127]\n\t"
        "ds_read_b128 v[148:151], v181 offset:10752\n\t"
        "v_exp_f32 v104, v104\n\t"
        "v_exp_f32 v105, v105\n\t"
        "v_exp_f32 v106, v106\n\t"
        "v_exp_f32 v107, v107\n\t"
        "v_smfmac_f32_32x32x32_bf16 v[128:143], v[152:155], v[96:103], v180\n\t"
        "ds_read_b128 v[152:155], v183 offset:14336\n\t"
        "v_exp_f32 v108, v108\n\t"
        "v_exp_f32 v109, v109\n\t"
        "v_exp_f32 v110, v110\n\t"
        "v_exp_f32 v111, v111\n\t"
        "s_waitcnt lgkmcnt(4)\n\t"
        "v_mfma_f32_32x32x8bf16_1k v[64:79], v[168:169], v[176:177], 0\n\t"
        "global_load_lds_dwordx4 %[wf3], s[22:23] offset:1024\n\t"
        "v_exp_f32 v112, v112\n\t"
        "v_exp_f32 v113, v113\n\t"
        "v_exp_f32 v114, v114\n\t"
        "v_exp_f32 v115, v115\n\t"
        "v_smfmac_f32_32x32x32_bf16 v[128:143], v[160:163], v[104:111], v180\n\t"
        "ds_read_b128 v[160:163], v183 offset:14976\n\t"
        "v_exp_f32 v116, v116\n\t"
        "v_exp_f32 v117, v117\n\t"
        "v_exp_f32 v118, v118\n\t"
        "v_exp_f32 v119, v119\n\t"
        "v_mfma_f32_32x32x16_bf16 v[64:79], v[144:147], v[172:175], v[64:79]\n\t"
        "ds_read_b128 v[144:147], v181 offset:11776\n\t"
        "v_exp_f32 v120, v120\n\t"
        "v_exp_f32 v121, v121\n\t"
        "v_exp_f32 v122, v122\n\t"
        "v_exp_f32 v123, v123\n\t"
        "v_smfmac_f32_32x32x32_bf16 v[128:143], v[156:159], v[112:119], v180\n\t"
        "ds_read_b128 v[156:159], v183 offset:15616\n\t"
        "v_exp_f32 v124, v124\n\t"
        "v_exp_f32 v125, v125\n\t"
        "v_exp_f32 v126, v126\n\t"
        "v_exp_f32 v127, v127\n\t"
        "s_waitcnt lgkmcnt(3)\n\t"
        "v_mfma_f32_32x32x8bf16_1k v[80:95], v[168:169], v[178:179], 0\n\t"
        "ds_read_b64 v[168:169], v182 offset:23552\n\t"
        "global_load_lds_dwordx4 %[wf4], s[22:23] offset:2048\n\t"
        "v_exp_f32 v64, v64\n\t"
        "v_exp_f32 v65, v65\n\t"
        "v_exp_f32 v66, v66\n\t"
        "v_exp_f32 v67, v67\n\t"
        "v_smfmac_f32_32x32x32_bf16 v[128:143], v[164:167], v[120:127], v180\n\t"
        "ds_read_b128 v[164:167], v183 offset:16256\n\t"
        "v_exp_f32 v68, v68\n\t"
        "v_exp_f32 v69, v69\n\t"
        "v_exp_f32 v70, v70\n\t"
        "v_exp_f32 v71, v71\n\t"
        "v_mfma_f32_32x32x16_bf16 v[80:95], v[148:151], v[172:175], v[80:95]\n\t"
        "ds_read_b128 v[148:151], v181 offset:12800\n\t"
        "v_exp_f32 v72, v72\n\t"
        "v_exp_f32 v73, v73\n\t"
        "v_exp_f32 v74, v74\n\t"
        "v_exp_f32 v75, v75\n\t"
        "v_smfmac_f32_32x32x32_bf16 v[128:143], v[152:155], v[64:71], v180\n\t"
        "ds_read_b128 v[152:155], v183 offset:16896\n\t"
        "v_exp_f32 v76, v76\n\t"
        "v_exp_f32 v77, v77\n\t"
        "v_exp_f32 v78, v78\n\t"
        "v_exp_f32 v79, v79\n\t"
        "s_add_u32 s22, s22, 294912\n\t"
        "s_addc_u32 s23, s23, 0\n\t"
        "s_add_u32 m0, m0, 40960\n\t"
        "s_waitcnt lgkmcnt(4)\n\t"
        "v_mfma_f32_32x32x8bf16_1k v[96:111], v[170:171], v[176:177], 0\n\t"
        "global_load_lds_dwordx4 %[wf0], s[22:23] offset:-2048\n\t"
        "v_exp_f32 v80, v80\n\t"
        "v_exp_f32 v81, v81\n\t"
        "v_exp_f32 v82, v82\n\t"
        "v_exp_f32 v83, v83\n\t"
        "v_smfmac_f32_32x32x32_bf16 v[128:143], v[160:163], v[72:79], v180\n\t"
        "ds_read_b128 v[160:163], v183 offset:17536\n\t"
        "v_exp_f32 v84, v84\n\t"
        "v_exp_f32 v85, v85\n\t"
        "v_exp_f32 v86, v86\n\t"
        "v_exp_f32 v87, v87\n\t"
        "v_mfma_f32_32x32x16_bf16 v[96:111], v[144:147], v[172:175], v[96:111]\n\t"
        "ds_read_b128 v[144:147], v181 offset:19456\n\t"
        "v_exp_f32 v88, v88\n\t"
        "v_exp_f32 v89, v89\n\t"
        "v_exp_f32 v90, v90\n\t"
        "v_exp_f32 v91, v91\n\t"
        "v_smfmac_f32_32x32x32_bf16 v[128:143], v[156:159], v[80:87], v180\n\t"
        "ds_read_b128 v[156:159], v183 offset:18176\n\t"
        "v_exp_f32 v92, v92\n\t"
        "v_exp_f32 v93, v93\n\t"
        "v_exp_f32 v94, v94\n\t"
        "v_exp_f32 v95, v95\n\t"
        "s_waitcnt lgkmcnt(3)\n\t"
        "v_mfma_f32_32x32x8bf16_1k v[112:127], v[170:171], v[178:179], 0\n\t"
        "ds_read_b64 v[170:171], v182 offset:23808\n\t"
        "global_load_lds_dwordx4 %[wf1], s[22:23] offset:-1024\n\t"
        "v_exp_f32 v96, v96\n\t"
        "v_exp_f32 v97, v97\n\t"
        "v_exp_f32 v98, v98\n\t"
        "v_exp_f32 v99, v99\n\t"
        "v_smfmac_f32_32x32x32_bf16 v[128:143], v[164:167], v[88:95], v180\n\t"
        "ds_read_b128 v[164:167], v183 offset:18816\n\t"
        "v_exp_f32 v100, v100\n\t"
        "v_exp_f32 v101, v101\n\t"
        "v_exp_f32 v102, v102\n\t"
        "v_exp_f32 v103, v103\n\t"
        "v_mfma_f32_32x32x16_bf16 v[112:127], v[148:151], v[172:175], v[112:127]\n\t"
        "ds_read_b128 v[148:151], v181 offset:20480\n\t"
        "v_exp_f32 v104, v104\n\t"
        "v_exp_f32 v105, v105\n\t"
        "v_exp_f32 v106, v106\n\t"
        "v_exp_f32 v107, v107\n\t"
        "v_smfmac_f32_32x32x32_bf16 v[128:143], v[152:155], v[96:103], v180\n\t"
        "ds_read_b128 v[152:155], v183 offset:24064\n\t"
        "v_exp_f32 v108, v108\n\t"
        "v_exp_f32 v109, v109\n\t"
        "v_exp_f32 v110, v110\n\t"
        "v_exp_f32 v111, v111\n\t"
        "s_waitcnt lgkmcnt(4)\n\t"
        "v_mfma_f32_32x32x8bf16_1k v[64:79], v[168:169], v[176:177], 0\n\t"
        "global_load_lds_dwordx4 %[wf2], s[22:23] offset:0\n\t"
        "v_exp_f32 v112, v112\n\t"
        "v_exp_f32 v113, v113\n\t"
        "v_exp_f32 v114, v114\n\t"
        "v_exp_f32 v115, v115\n\t"
        "v_smfmac_f32_32x32x32_bf16 v[128:143], v[160:163], v[104:111], v180\n\t"
        "ds_read_b128 v[160:163], v183 offset:24704\n\t"
        "v_exp_f32 v116, v116\n\t"
        "v_exp_f32 v117, v117\n\t"
        "v_exp_f32 v118, v118\n\t"
        "v_exp_f32 v119, v119\n\t"
        "v_mfma_f32_32x32x16_bf16 v[64:79], v[144:147], v[172:175], v[64:79]\n\t"
        "ds_read_b128 v[144:147], v181 offset:21504\n\t"
        "v_exp_f32 v120, v120\n\t"
        "v_exp_f32 v121, v121\n\t"
        "v_exp_f32 v122, v122\n\t"
        "v_exp_f32 v123, v123\n\t"
        "v_smfmac_f32_32x32x32_bf16 v[128:143], v[156:159], v[112:119], v180\n\t"
        "ds_read_b128 v[156:159], v183 offset:25344\n\t"
        "v_exp_f32 v124, v124\n\t"
        "v_exp_f32 v125, v125\n\t"
        "v_exp_f32 v126, v126\n\t"
        "v_exp_f32 v127, v127\n\t"
        "s_waitcnt lgkmcnt(3)\n\t"
        "v_mfma_f32_32x32x8bf16_1k v[80:95], v[168:169], v[178:179], 0\n\t"
        "ds_read_b64 v[168:169], v182 offset:33280\n\t"
        "global_load_lds_dwordx4 %[wf3], s[22:23] offset:1024\n\t"
        "v_exp_f32 v64, v64\n\t"
        "v_exp_f32 v65, v65\n\t"
        "v_exp_f32 v66, v66\n\t"
        "v_exp_f32 v67, v67\n\t"
        "v_smfmac_f32_32x32x32_bf16 v[128:143], v[164:167], v[120:127], v180\n\t"
        "ds_read_b128 v[164:167], v183 offset:25984\n\t"
        "v_exp_f32 v68, v68\n\t"
        "v_exp_f32 v69, v69\n\t"
        "v_exp_f32 v70, v70\n\t"
        "v_exp_f32 v71, v71\n\t"
        "v_mfma_f32_32x32x16_bf16 v[80:95], v[148:151], v[172:175], v[80:95]\n\t"
        "ds_read_b128 v[148:151], v181 offset:22528\n\t"
        "v_exp_f32 v72, v72\n\t"
        "v_exp_f32 v73, v73\n\t"
        "v_exp_f32 v74, v74\n\t"
        "v_exp_f32 v75, v75\n\t"
        "v_smfmac_f32_32x32x32_bf16 v[128:143], v[152:155], v[64:71], v180\n\t"
        "ds_read_b128 v[152:155], v183 offset:26624\n\t"
        "v_exp_f32 v76, v76\n\t"
        "v_exp_f32 v77, v77\n\t"
        "v_exp_f32 v78, v78\n\t"
        "v_exp_f32 v79, v79\n\t"
        "s_waitcnt lgkmcnt(4)\n\t"
        "v_mfma_f32_32x32x8bf16_1k v[96:111], v[170:171], v[176:177], 0\n\t"
        "global_load_lds_dwordx4 %[wf4], s[22:23] offset:2048\n\t"
        "v_exp_f32 v80, v80\n\t"
        "v_exp_f32 v81, v81\n\t"
        "v_exp_f32 v82, v82\n\t"
        "v_exp_f32 v83, v83\n\t"
        "v_smfmac_f32_32x32x32_bf16 v[128:143], v[160:163], v[72:79], v180\n\t"
        "ds_read_b128 v[160:163], v183 offset:27264\n\t"
        "v_exp_f32 v84, v84\n\t"
        "v_exp_f32 v85, v85\n\t"
        "v_exp_f32 v86, v86\n\t"
        "v_exp_f32 v87, v87\n\t"
        "v_mfma_f32_32x32x16_bf16 v[96:111], v[144:147], v[172:175], v[96:111]\n\t"
        "ds_read_b128 v[144:147], v181 offset:29184\n\t"
        "v_exp_f32 v88, v88\n\t"
        "v_exp_f32 v89, v89\n\t"
        "v_exp_f32 v90, v90\n\t"
        "v_exp_f32 v91, v91\n\t"
        "v_smfmac_f32_32x32x32_bf16 v[128:143], v[156:159], v[80:87], v180\n\t"
        "ds_read_b128 v[156:159], v183 offset:27904\n\t"
        "v_exp_f32 v92, v92\n\t"
        "v_exp_f32 v93, v93\n\t"
        "v_exp_f32 v94, v94\n\t"
        "v_exp_f32 v95, v95\n\t"
        "s_mov_b32 m0, %[xm0a]\n\t"
        "s_waitcnt lgkmcnt(3)\n\t"
        "v_mfma_f32_32x32x8bf16_1k v[112:127], v[170:171], v[178:179], 0\n\t"
        "ds_read_b64 v[170:171], v182 offset:33536\n\t"
        "global_load_lds_dwordx4 %[wx1], %[wob]\n\t"
        "v_exp_f32 v96, v96\n\t"
        "v_exp_f32 v97, v97\n\t"
        "v_exp_f32 v98, v98\n\t"
        "v_exp_f32 v99, v99\n\t"
        "v_smfmac_f32_32x32x32_bf16 v[128:143], v[164:167], v[88:95], v180\n\t"
        "ds_read_b128 v[164:167], v183 offset:28544\n\t"
        "v_exp_f32 v100, v100\n\t"
        "v_exp_f32 v101, v101\n\t"
        "v_exp_f32 v102, v102\n\t"
        "v_exp_f32 v103, v103\n\t"
        "v_mfma_f32_32x32x16_bf16 v[112:127], v[148:151], v[172:175], v[112:127]\n\t"
        "ds_read_b128 v[148:151], v181 offset:30208\n\t"
        "v_exp_f32 v104, v104\n\t"
        "v_exp_f32 v105, v105\n\t"
        "v_exp_f32 v106, v106\n\t"
        "v_exp_f32 v107, v107\n\t"
        "v_smfmac_f32_32x32x32_bf16 v[128:143], v[152:155], v[96:103], v180\n\t"
        "ds_read_b128 v[152:155], v183 offset:33792\n\t"
        "v_exp_f32 v108, v108\n\t"
        "v_exp_f32 v109, v109\n\t"
        "v_exp_f32 v110, v110\n\t"
        "v_exp_f32 v111, v111\n\t"
        "s_mov_b32 m0, %[xm0b]\n\t"
        "s_waitcnt lgkmcnt(4)\n\t"
        "v_mfma_f32_32x32x8bf16_1k v[64:79], v[168:169], v[176:177], 0\n\t"
        "global_load_lds_dwordx4 %[wx2], %[wob]\n\t"
        "v_exp_f32 v112, v112\n\t"
        "v_exp_f32 v113, v113\n\t"
        "v_exp_f32 v114, v114\n\t"
        "v_exp_f32 v115, v115\n\t"
        "v_smfmac_f32_32x32x32_bf16 v[128:143], v[160:163], v[104:111], v180\n\t"
        "ds_read_b128 v[160:163], v183 offset:34432\n\t"
        "v_exp_f32 v116, v116\n\t"
        "v_exp_f32 v117, v117\n\t"
        "v_exp_f32 v118, v118\n\t"
        "v_exp_f32 v119, v119\n\t"
        "v_mfma_f32_32x32x16_bf16 v[64:79], v[144:147], v[172:175], v[64:79]\n\t"
        "ds_read_b128 v[144:147], v181 offset:31232\n\t"
        "v_exp_f32 v120, v120\n\t"
        "v_exp_f32 v121, v121\n\t"
        "v_exp_f32 v122, v122\n\t"
        "v_exp_f32 v123, v123\n\t"
        "v_smfmac_f32_32x32x32_bf16 v[128:143], v[156:159], v[112:119], v180\n\t"
        "ds_read_b128 v[156:159], v183 offset:35072\n\t"
        "v_exp_f32 v124, v124\n\t"
        "v_exp_f32 v125, v125\n\t"
        "v_exp_f32 v126, v126\n\t"
        "v_exp_f32 v127, v127\n\t"
        "s_waitcnt lgkmcnt(3)\n\t"
        "v_mfma_f32_32x32x8bf16_1k v[80:95], v[168:169], v[178:179], 0\n\t"
        "ds_read_b64 v[168:169], v182 offset:43008\n\t"
        "v_exp_f32 v64, v64\n\t"
        "v_exp_f32 v65, v65\n\t"
        "v_exp_f32 v66, v66\n\t"
        "v_exp_f32 v67, v67\n\t"
        "v_smfmac_f32_32x32x32_bf16 v[128:143], v[164:167], v[120:127], v180\n\t"
        "ds_read_b128 v[164:167], v183 offset:35712\n\t"
        "v_exp_f32 v68, v68\n\t"
        "v_exp_f32 v69, v69\n\t"
        "v_exp_f32 v70, v70\n\t"
        "v_exp_f32 v71, v71\n\t"
        "v_mfma_f32_32x32x16_bf16 v[80:95], v[148:151], v[172:175], v[80:95]\n\t"
        "ds_read_b128 v[148:151], v181 offset:32256\n\t"
        "v_exp_f32 v72, v72\n\t"
        "v_exp_f32 v73, v73\n\t"
        "v_exp_f32 v74, v74\n\t"
        "v_exp_f32 v75, v75\n\t"
        "v_smfmac_f32_32x32x32_bf16 v[128:143], v[152:155], v[64:71], v180\n\t"
        "ds_read_b128 v[152:155], v183 offset:36352\n\t"
        "v_exp_f32 v76, v76\n\t"
        "v_exp_f32 v77, v77\n\t"
        "v_exp_f32 v78, v78\n\t"
        "v_exp_f32 v79, v79\n\t"
        "s_waitcnt lgkmcnt(4)\n\t"
        "v_mfma_f32_32x32x8bf16_1k v[96:111], v[170:171], v[176:177], 0\n\t"
        "v_exp_f32 v80, v80\n\t"
        "v_exp_f32 v81, v81\n\t"
        "v_exp_f32 v82, v82\n\t"
        "v_exp_f32 v83, v83\n\t"
        "v_smfmac_f32_32x32x32_bf16 v[128:143], v[160:163], v[72:79], v180\n\t"
        "ds_read_b128 v[160:163], v183 offset:36992\n\t"
        "v_exp_f32 v84, v84\n\t"
        "v_exp_f32 v85, v85\n\t"
        "v_exp_f32 v86, v86\n\t"
        "v_exp_f32 v87, v87\n\t"
        "v_mfma_f32_32x32x16_bf16 v[96:111], v[144:147], v[172:175], v[96:111]\n\t"
        "ds_read_b128 v[144:147], v181 offset:38912\n\t"
        "v_exp_f32 v88, v88\n\t"
        "v_exp_f32 v89, v89\n\t"
        "v_exp_f32 v90, v90\n\t"
        "v_exp_f32 v91, v91\n\t"
        "v_smfmac_f32_32x32x32_bf16 v[128:143], v[156:159], v[80:87], v180\n\t"
        "ds_read_b128 v[156:159], v183 offset:37632\n\t"
        "v_exp_f32 v92, v92\n\t"
        "v_exp_f32 v93, v93\n\t"
        "v_exp_f32 v94, v94\n\t"
        "v_exp_f32 v95, v95\n\t"
        "s_waitcnt lgkmcnt(3)\n\t"
        "v_mfma_f32_32x32x8bf16_1k v[112:127], v[170:171], v[178:179], 0\n\t"
        "ds_read_b64 v[170:171], v182 offset:43264\n\t"
        "v_exp_f32 v96, v96\n\t"
        "v_exp_f32 v97, v97\n\t"
        "v_exp_f32 v98, v98\n\t"
        "v_exp_f32 v99, v99\n\t"
        "v_smfmac_f32_32x32x32_bf16 v[128:143], v[164:167], v[88:95], v180\n\t"
        "ds_read_b128 v[164:167], v183 offset:38272\n\t"
        "v_exp_f32 v100, v100\n\t"
        "v_exp_f32 v101, v101\n\t"
        "v_exp_f32 v102, v102\n\t"
        "v_exp_f32 v103, v103\n\t"
        "v_mfma_f32_32x32x16_bf16 v[112:127], v[148:151], v[172:175], v[112:127]\n\t"
        "ds_read_b128 v[148:151], v181 offset:39936\n\t"
        "v_exp_f32 v104, v104\n\t"
        "v_exp_f32 v105, v105\n\t"
        "v_exp_f32 v106, v106\n\t"
        "v_exp_f32 v107, v107\n\t"
        "v_smfmac_f32_32x32x32_bf16 v[128:143], v[152:155], v[96:103], v180\n\t"
        "ds_read_b128 v[152:155], v183 offset:43520\n\t"
        "v_exp_f32 v108, v108\n\t"
        "v_exp_f32 v109, v109\n\t"
        "v_exp_f32 v110, v110\n\t"
        "v_exp_f32 v111, v111\n\t"
        "s_waitcnt lgkmcnt(4)\n\t"
        "v_mfma_f32_32x32x8bf16_1k v[64:79], v[168:169], v[176:177], 0\n\t"
        "v_exp_f32 v112, v112\n\t"
        "v_exp_f32 v113, v113\n\t"
        "v_exp_f32 v114, v114\n\t"
        "v_exp_f32 v115, v115\n\t"
        "v_smfmac_f32_32x32x32_bf16 v[128:143], v[160:163], v[104:111], v180\n\t"
        "ds_read_b128 v[160:163], v183 offset:44160\n\t"
        "v_exp_f32 v116, v116\n\t"
        "v_exp_f32 v117, v117\n\t"
        "v_exp_f32 v118, v118\n\t"
        "v_exp_f32 v119, v119\n\t"
        "v_mfma_f32_32x32x16_bf16 v[64:79], v[144:147], v[172:175], v[64:79]\n\t"
        "ds_read_b128 v[144:147], v181 offset:40960\n\t"
        "v_exp_f32 v120, v120\n\t"
        "v_exp_f32 v121, v121\n\t"
        "v_exp_f32 v122, v122\n\t"
        "v_exp_f32 v123, v123\n\t"
        "v_smfmac_f32_32x32x32_bf16 v[128:143], v[156:159], v[112:119], v180\n\t"
        "ds_read_b128 v[156:159], v183 offset:44800\n\t"
        "v_exp_f32 v124, v124\n\t"
        "v_exp_f32 v125, v125\n\t"
        "v_exp_f32 v126, v126\n\t"
        "v_exp_f32 v127, v127\n\t"
        "s_waitcnt lgkmcnt(0)\n\t"
        "v_smfmac_f32_32x32x32_bf16 v[128:143], v[164:167], v[120:127], v180\n\t"
        "s_mov_b32 m0, s21\n\t"
        "s_setprio 0\n\t"
        "s_nop 15\n\t"
        "s_nop 3\n\t"
        : "=&{v[128:143]}"(o), "=&{v[172:175]}"(qm), "=&{v176}"(ql)
        : [voff] "v"(lane * 16), [qa] "v"(as.qa), [qla] "v"(as.qla), [kvp] "s"(kvp), [m0b] "s"(m0base), [wv] "s"(w),
          [hm] "v"(as.hm), [aK] "v"(as.aK), [aL] "v"(as.aL), [aV] "v"(as.aV),
          [wf0] "v"(as.wf[0]), [wf1] "v"(as.wf[1]), [wf2] "v"(as.wf[2]), [wf3] "v"(as.wf[3]), [wf4] "v"(as.wf[4]), [wx1] "v"(as.wx[0]), [wx2] "v"(as.wx[1]),
          [wob] "s"(wob), [wbase] "s"(wbase), [wm0] "s"(uni(lds + w * 5120 + 2048)), [xm0a] "s"(xm0[0]), [xm0b] "s"(xm0[1])
        : "memory", "scc", "vcc", "s20", "s21", "s22", "s23", "s24",
          "v64", "v65", "v66", "v67", "v68", "v69", "v70", "v71", "v72", "v73", "v74", "v75", "v76", "v77", "v78", "v79", "v80", "v81", "v82", "v83", "v84", "v85", "v86", "v87", "v88", "v89", "v90", "v91", "v92", "v93", "v94", "v95", "v96", "v97", "v98", "v99", "v100", "v101", "v102", "v103", "v104", "v105", "v106", "v107", "v108", "v109", "v110", "v111", "v112", "v113", "v114", "v115", "v116", "v117", "v118", "v119", "v120", "v121", "v122", "v123", "v124", "v125", "v126", "v127", "v144", "v145", "v146", "v147", "v148", "v149", "v150", "v151", "v152", "v153", "v154", "v155", "v156", "v157", "v158", "v159", "v160", "v161", "v162", "v163", "v164", "v165", "v166", "v167", "v168", "v169", "v170", "v171", "v177", "v178", "v179", "v180", "v181", "v182", "v183", "v184"
    );
    auto emit = [&]() {
        const float inv = __builtin_amdgcn_rcpf(o[9]);
        u32x4 v;
#pragma unroll
        for (int j = 0; j < 4; ++j) v[j] = pk2(o[2 * j] * inv, o[2 * j + 1] * inv);
        st16(ws, as.omo, v);
        const u32 mine = __float_as_uint(o[8] * inv);
        const auto lr = __builtin_amdgcn_permlane32_swap(mine, mine, false, false);
        st4(ws, as.olo, pk2(__uint_as_float(lr[0]), __uint_as_float(lr[1])));
    };
    emit();
    asm volatile("" ::: "memory");
    float sa = fabsf(o[0]);
#pragma unroll
    for (int i = 1; i < 9; ++i) sa += fabsf(o[i]);
    const bool ok = o[9] > 8.67e-19f && o[9] < 1.15e18f && sa < 3.0e38f;
    if (!__all(ok)) {
        const u32 img = WS_KV + h * KVIMG - lds;
        o = attn_true_max(ws, as.aK + img, as.aL == OOR ? OOB : as.aL + img, as.aV == OOR ? OOB : as.aV + img, qm, ql);
        WAITV(0);
        emit();
    }
    u64 e0 = 0, e1 = 0, e2 = 0, e3 = 0;
    const char* fb = uni64(p.ws + WS_F);
    const bool bump = b == 0 && w == 7;
    if (bump)
        asm volatile("global_load_dwordx2 %0, %4, %5 sc1\n\tglobal_load_dwordx2 %1, %4, %5 offset:512 sc1\n\t"
                     "global_load_dwordx2 %2, %4, %5 offset:1024 sc1\n\tglobal_load_dwordx2 %3, %4, %5 offset:1536 sc1"
                     : "=&v"(e0), "=&v"(e1), "=&v"(e2), "=&v"(e3) : "v"(FL_F1 + lane * 8), "s"(fb) : "memory");
    WAITV(0);
    BAR();
    st8(ws, tid < 2 ? WS_F + (tid ? FL_F3B + b * 8 : FL_F3 + (qb * 32 + h) * 8) : OOB, tag);
    if (bump) {
        asm volatile("" : "+v"(e0), "+v"(e1), "+v"(e2), "+v"(e3));
        if (!__all(e0 == tag && e1 == tag && e2 == tag && e3 == tag))
            for (int i = 0; i < 4; ++i) wait_flags(fb, FL_F1 + (i * 64 + lane) * 8, tag);
        st8(ws, lane == 0 ? WS_F : OOB, tag);
    }
}

constexpr int WO_ROW = 160, WO_BYTES = 576 * WO_ROW, Y_OFF = WO_BYTES, RED4_OFF = Y_OFF + 72 * 128;

__device__ __forceinline__ void phase_out(const Ptrs& p, rsrc_t ws, int b, u32 lds, u64 tag) {
    const int tid = threadIdx.x, lane = tid & 63;
    const int w = uni(tid >> 6);
    const int xcd = b & 7, ii = b >> 3;
    const int tb = xcd * 4 + (ii >> 3), ng = ii & 7;
    const int tt = w & 3, kh = w >> 2;
    const int t16 = tb * 4 + tt, r = lane & 15, g = lane >> 4, t = t16 * 16 + r;
    u32x2 xv[3];
    u32 oo[3], ya[3];
#pragma unroll
    for (int j = 0; j < 3; ++j) {
        const int nt = kh ? 3 + j : j, n = 16 * nt + 4 * g, yr = n + (r >> 2);
        const bool act = n < 72;
        xv[j] = *(const u32x2*)(p.x + (size_t)t * DM + ng * 72 + (act ? n : 0));
        oo[j] = act ? (t * DM + ng * 72 + n) * 2 : OOB;
        ya[j] = yr < 72 ? lds + Y_OFF + yr * 128 + tt * 32 + (r & 3) * 8 : OOR;
    }
    const u32 wa = lds + (36 * 9 * kh + 20 * (g >> 1) + 4 * (g & 1) + (r >> 2)) * WO_ROW + (r & 3) * 8;
    const u32 wl = lds + (72 * g + 16 + (r >> 2)) * WO_ROW + (r & 3) * 8;
    const u32 fo = WS_F + (lane < 32 ? FL_F3 + ((tb >> 2) * 32 + lane) * 8 : FL_F3B + (32 * ng + lane - 32) * 8);
    const u32 om = WS_OM + (t16 * 16 + 9 * kh) * 1024 + lane * 16 + 4096;
    const u32 ol = WS_OL + (4 * g * SEQ + t) * 4;
    const u32 yo = WS_YT + (ng * 72 + w * 8 + (lane >> 3)) * 4096 + tb * 128 + (lane & 7) * 16;
    const u32 yo1 = w == 0 ? yo + 64 * 4096 : yo;
    const u32 ym0 = uni(lds + Y_OFF + w * 1024), ym1 = uni(w == 0 ? lds + Y_OFF + 8192 : lds + Y_OFF + w * 1024);
    const char* wsb = uni64(p.ws);
    const u32 wsl = uni((u32)(uint64_t)wsb), wsh = uni((u32)((uint64_t)wsb >> 32));
    f32x16 a03; f32x4 a4; float gate;
    u32 red = lds + RED4_OFF + tt * 5120 + lane * 16;
    asm volatile("" : "+v"(oo[0]), "+v"(oo[1]), "+v"(oo[2]), "+v"(ya[0]), "+v"(ya[1]), "+v"(ya[2]), "+v"(red));
    const rsrc_t ro = __builtin_amdgcn_make_buffer_rsrc(p.out, 0, SEQ * DM * 2, 0x00020000);
    auto mix = [&](int j, f32x4 a, s16x4 y) {
        float o[4];
#pragma unroll
        for (int e = 0; e < 4; ++e) {
            const float xe = (e & 1) ? bfhi(xv[j][e >> 1]) : bflo(xv[j][e >> 1]);
            const float ye = __uint_as_float((u32)(u16)y[e] << 16);
            o[e] = gate * a[e] + (1.f - gate) * (xe * ye);
        }
        const u32x2 v = {pk2(o[0], o[1]), pk2(o[2], o[3])};
        __builtin_amdgcn_raw_buffer_store_b64(v, ro, oo[j], 0, 0);
    };
    auto tile = [&](int n) { return n < 4 ? f32x4{a03[4 * n], a03[4 * n + 1], a03[4 * n + 2], a03[4 * n + 3]} : a4; };
#define P4_OPS                                                                                                                       \
        : "=&{v[240:255]}"(a03), "=&{v[60:63]}"(a4), "=&{v59}"(gate)                                                                \
        : [wa] "v"(wa), [wl] "v"(wl), [wv] "s"(w), [fo] "v"(fo), [ws] "s"(wsb), [wsl] "s"(wsl), [wsh] "s"(wsh), [tag] "v"(tag),     \
          [om] "v"(om), [ol] "v"(ol), [go] "v"(WS_G + t * 4), [yo] "v"(yo), [yo1] "v"(yo1), [ym0] "s"(ym0), [ym1] "s"(ym1)
    if (kh == 0) {
        asm volatile(
        "s_mov_b32 s21, m0\n\t"
        "ds_read_b64_tr_b16 v[64:65], %[wa] offset:0\n\t"
        "ds_read_b64_tr_b16 v[66:67], %[wa] offset:1280\n\t"
        "ds_read_b64_tr_b16 v[68:69], %[wa] offset:32\n\t"
        "ds_read_b64_tr_b16 v[70:71], %[wa] offset:1312\n\t"
        "ds_read_b64_tr_b16 v[72:73], %[wa] offset:64\n\t"
        "ds_read_b64_tr_b16 v[74:75], %[wa] offset:1344\n\t"
        "ds_read_b64_tr_b16 v[76:77], %[wa] offset:96\n\t"
        "ds_read_b64_tr_b16 v[78:79], %[wa] offset:1376\n\t"
        "ds_read_b64_tr_b16 v[80:81], %[wa] offset:128\n\t"
        "ds_read_b64_tr_b16 v[82:83], %[wa] offset:1408\n\t"
        "ds_read_b64_tr_b16 v[84:85], %[wa] offset:5760\n\t"
        "ds_read_b64_tr_b16 v[86:87], %[wa] offset:7040\n\t"
        "ds_read_b64_tr_b16 v[88:89], %[wa] offset:5792\n\t"
        "ds_read_b64_tr_b16 v[90:91], %[wa] offset:7072\n\t"
        "ds_read_b64_tr_b16 v[92:93], %[wa] offset:5824\n\t"
        "ds_read_b64_tr_b16 v[94:95], %[wa] offset:7104\n\t"
        "ds_read_b64_tr_b16 v[96:97], %[wa] offset:5856\n\t"
        "ds_read_b64_tr_b16 v[98:99], %[wa] offset:7136\n\t"
        "ds_read_b64_tr_b16 v[100:101], %[wa] offset:5888\n\t"
        "ds_read_b64_tr_b16 v[102:103], %[wa] offset:7168\n\t"
        "ds_read_b64_tr_b16 v[104:105], %[wa] offset:11520\n\t"
        "ds_read_b64_tr_b16 v[106:107], %[wa] offset:12800\n\t"
        "ds_read_b64_tr_b16 v[108:109], %[wa] offset:11552\n\t"
        "ds_read_b64_tr_b16 v[110:111], %[wa] offset:12832\n\t"
        "ds_read_b64_tr_b16 v[112:113], %[wa] offset:11584\n\t"
        "ds_read_b64_tr_b16 v[114:115], %[wa] offset:12864\n\t"
        "ds_read_b64_tr_b16 v[116:117], %[wa] offset:11616\n\t"
        "ds_read_b64_tr_b16 v[118:119], %[wa] offset:12896\n\t"
        "ds_read_b64_tr_b16 v[120:121], %[wa] offset:11648\n\t"
        "ds_read_b64_tr_b16 v[122:123], %[wa] offset:12928\n\t"
        "ds_read_b64_tr_b16 v[124:125], %[wa] offset:17280\n\t"
        "ds_read_b64_tr_b16 v[126:127], %[wa] offset:18560\n\t"
        "ds_read_b64_tr_b16 v[128:129], %[wa] offset:17312\n\t"
        "ds_read_b64_tr_b16 v[130:131], %[wa] offset:18592\n\t"
        "ds_read_b64_tr_b16 v[132:133], %[wa] offset:17344\n\t"
        "ds_read_b64_tr_b16 v[134:135], %[wa] offset:18624\n\t"
        "ds_read_b64_tr_b16 v[136:137], %[wa] offset:17376\n\t"
        "ds_read_b64_tr_b16 v[138:139], %[wa] offset:18656\n\t"
        "ds_read_b64_tr_b16 v[140:141], %[wa] offset:17408\n\t"
        "ds_read_b64_tr_b16 v[142:143], %[wa] offset:18688\n\t"
        "ds_read_b64_tr_b16 v[144:145], %[wa] offset:23040\n\t"
        "ds_read_b64_tr_b16 v[146:147], %[wa] offset:24320\n\t"
        "ds_read_b64_tr_b16 v[148:149], %[wa] offset:23072\n\t"
        "ds_read_b64_tr_b16 v[150:151], %[wa] offset:24352\n\t"
        "ds_read_b64_tr_b16 v[152:153], %[wa] offset:23104\n\t"
        "ds_read_b64_tr_b16 v[154:155], %[wa] offset:24384\n\t"
        "ds_read_b64_tr_b16 v[156:157], %[wa] offset:23136\n\t"
        "ds_read_b64_tr_b16 v[158:159], %[wa] offset:24416\n\t"
        "ds_read_b64_tr_b16 v[160:161], %[wa] offset:23168\n\t"
        "ds_read_b64_tr_b16 v[162:163], %[wa] offset:24448\n\t"
        "ds_read_b64_tr_b16 v[164:165], %[wa] offset:28800\n\t"
        "ds_read_b64_tr_b16 v[166:167], %[wa] offset:30080\n\t"
        "ds_read_b64_tr_b16 v[168:169], %[wa] offset:28832\n\t"
        "ds_read_b64_tr_b16 v[170:171], %[wa] offset:30112\n\t"
        "ds_read_b64_tr_b16 v[172:173], %[wa] offset:28864\n\t"
        "ds_read_b64_tr_b16 v[174:175], %[wa] offset:30144\n\t"
        "ds_read_b64_tr_b16 v[176:177], %[wa] offset:28896\n\t"
        "ds_read_b64_tr_b16 v[178:179], %[wa] offset:30176\n\t"
        "ds_read_b64_tr_b16 v[180:181], %[wa] offset:28928\n\t"
        "ds_read_b64_tr_b16 v[182:183], %[wa] offset:30208\n\t"
        "ds_read_b64_tr_b16 v[184:185], %[wa] offset:34560\n\t"
        "ds_read_b64_tr_b16 v[186:187], %[wa] offset:35840\n\t"
        "ds_read_b64_tr_b16 v[188:189], %[wa] offset:34592\n\t"
        "ds_read_b64_tr_b16 v[190:191], %[wa] offset:35872\n\t"
        "ds_read_b64_tr_b16 v[192:193], %[wa] offset:34624\n\t"
        "ds_read_b64_tr_b16 v[194:195], %[wa] offset:35904\n\t"
        "ds_read_b64_tr_b16 v[196:197], %[wa] offset:34656\n\t"
        "ds_read_b64_tr_b16 v[198:199], %[wa] offset:35936\n\t"
        "ds_read_b64_tr_b16 v[200:201], %[wa] offset:34688\n\t"
        "ds_read_b64_tr_b16 v[202:203], %[wa] offset:35968\n\t"
        "v_mov_b32 v240, 0\n\t"
        "v_mov_b32 v241, 0\n\t"
        "v_mov_b32 v242, 0\n\t"
        "v_mov_b32 v243, 0\n\t"
        "v_mov_b32 v244, 0\n\t"
        "v_mov_b32 v245, 0\n\t"
        "v_mov_b32 v246, 0\n\t"
        "v_mov_b32 v247, 0\n\t"
        "v_mov_b32 v248, 0\n\t"
        "v_mov_b32 v249, 0\n\t"
        "v_mov_b32 v250, 0\n\t"
        "v_mov_b32 v251, 0\n\t"
        "v_mov_b32 v252, 0\n\t"
        "v_mov_b32 v253, 0\n\t"
        "v_mov_b32 v254, 0\n\t"
        "v_mov_b32 v255, 0\n\t"
        "v_mov_b32 v60, 0\n\t"
        "v_mov_b32 v61, 0\n\t"
        "v_mov_b32 v62, 0\n\t"
        "v_mov_b32 v63, 0\n\t"
        "s_cmp_lg_u32 %[wv], 0\n\t"
        "s_cbranch_scc1 2f\n\t"
        "s_mov_b32 s20, 40000\n\t"
        "1:\n\t"
        "global_load_dwordx2 v[56:57], %[fo], %[ws] sc1\n\t"
        "s_waitcnt vmcnt(0)\n\t"
        "v_cmp_eq_u64 vcc, v[56:57], %[tag]\n\t"
        "s_cmp_eq_u64 vcc, exec\n\t"
        "s_cbranch_scc1 2f\n\t"
        "s_sleep 1\n\t"
        "s_sub_u32 s20, s20, 1\n\t"
        "s_cmp_lg_u32 s20, 0\n\t"
        "s_cbranch_scc1 1b\n\t"
        "2:\n\t"
        "s_barrier\n\t"
        "global_load_dwordx4 v[204:207], %[om], %[ws] offset:-4096 sc1\n\t"
        "global_load_dwordx4 v[208:211], %[om], %[ws] offset:-3072 sc1\n\t"
        "global_load_dwordx4 v[212:215], %[om], %[ws] offset:-2048 sc1\n\t"
        "global_load_dwordx4 v[216:219], %[om], %[ws] offset:-1024 sc1\n\t"
        "global_load_dwordx4 v[220:223], %[om], %[ws] offset:0 sc1\n\t"
        "global_load_dwordx4 v[224:227], %[om], %[ws] offset:1024 sc1\n\t"
        "global_load_dwordx4 v[228:231], %[om], %[ws] offset:2048 sc1\n\t"
        "global_load_dwordx4 v[232:235], %[om], %[ws] offset:3072 sc1\n\t"
        "v_add_u32 v55, 4096, %[om]\n\t"
        "global_load_dwordx4 v[236:239], v55, %[ws] sc1\n\t"
        "global_load_dword v59, %[go], %[ws] sc1\n\t"
        "s_mov_b32 m0, %[ym0]\n\t"
        "s_nop 0\n\t"
        "global_load_lds_dwordx4 %[yo], %[ws] sc1\n\t"
        "s_mov_b32 m0, %[ym1]\n\t"
        "s_nop 0\n\t"
        "global_load_lds_dwordx4 %[yo1], %[ws] sc1\n\t"
        "s_mov_b32 m0, s21\n\t"
        "s_waitcnt lgkmcnt(0)\n\t"
        "s_waitcnt vmcnt(11)\n\t"
        "v_mfma_f32_16x16x32_bf16 v[240:243], v[64:67], v[204:207], v[240:243]\n\t"
        "v_mfma_f32_16x16x32_bf16 v[244:247], v[68:71], v[204:207], v[244:247]\n\t"
        "v_mfma_f32_16x16x32_bf16 v[248:251], v[72:75], v[204:207], v[248:251]\n\t"
        "v_mfma_f32_16x16x32_bf16 v[252:255], v[76:79], v[204:207], v[252:255]\n\t"
        "v_mfma_f32_16x16x32_bf16 v[60:63], v[80:83], v[204:207], v[60:63]\n\t"
        "ds_read_b64_tr_b16 v[64:65], %[wa] offset:40320\n\t"
        "ds_read_b64_tr_b16 v[66:67], %[wa] offset:41600\n\t"
        "ds_read_b64_tr_b16 v[68:69], %[wa] offset:40352\n\t"
        "ds_read_b64_tr_b16 v[70:71], %[wa] offset:41632\n\t"
        "ds_read_b64_tr_b16 v[72:73], %[wa] offset:40384\n\t"
        "ds_read_b64_tr_b16 v[74:75], %[wa] offset:41664\n\t"
        "ds_read_b64_tr_b16 v[76:77], %[wa] offset:40416\n\t"
        "ds_read_b64_tr_b16 v[78:79], %[wa] offset:41696\n\t"
        "ds_read_b64_tr_b16 v[80:81], %[wa] offset:40448\n\t"
        "ds_read_b64_tr_b16 v[82:83], %[wa] offset:41728\n\t"
        "s_waitcnt vmcnt(10)\n\t"
        "v_mfma_f32_16x16x32_bf16 v[240:243], v[84:87], v[208:211], v[240:243]\n\t"
        "v_mfma_f32_16x16x32_bf16 v[244:247], v[88:91], v[208:211], v[244:247]\n\t"
        "v_mfma_f32_16x16x32_bf16 v[248:251], v[92:95], v[208:211], v[248:251]\n\t"
        "v_mfma_f32_16x16x32_bf16 v[252:255], v[96:99], v[208:211], v[252:255]\n\t"
        "v_mfma_f32_16x16x32_bf16 v[60:63], v[100:103], v[208:211], v[60:63]\n\t"
        "ds_read_b64_tr_b16 v[84:85], %[wa] offset:46080\n\t"
        "ds_read_b64_tr_b16 v[86:87], %[wa] offset:47360\n\t"
        "ds_read_b64_tr_b16 v[88:89], %[wa] offset:46112\n\t"
        "ds_read_b64_tr_b16 v[90:91], %[wa] offset:47392\n\t"
        "ds_read_b64_tr_b16 v[92:93], %[wa] offset:46144\n\t"
        "ds_read_b64_tr_b16 v[94:95], %[wa] offset:47424\n\t"
        "ds_read_b64_tr_b16 v[96:97], %[wa] offset:46176\n\t"
        "ds_read_b64_tr_b16 v[98:99], %[wa] offset:47456\n\t"
        "ds_read_b64_tr_b16 v[100:101], %[wa] offset:46208\n\t"
        "ds_read_b64_tr_b16 v[102:103], %[wa] offset:47488\n\t"
        "s_waitcnt vmcnt(9)\n\t"
        "v_mfma_f32_16x16x32_bf16 v[240:243], v[104:107], v[212:215], v[240:243]\n\t"
        "v_mfma_f32_16x16x32_bf16 v[244:247], v[108:111], v[212:215], v[244:247]\n\t"
        "v_mfma_f32_16x16x32_bf16 v[248:251], v[112:115], v[212:215], v[248:251]\n\t"
        "v_mfma_f32_16x16x32_bf16 v[252:255], v[116:119], v[212:215], v[252:255]\n\t"
        "v_mfma_f32_16x16x32_bf16 v[60:63], v[120:123], v[212:215], v[60:63]\n\t"
        "s_waitcnt vmcnt(8)\n\t"
        "v_mfma_f32_16x16x32_bf16 v[240:243], v[124:127], v[216:219], v[240:243]\n\t"
        "v_mfma_f32_16x16x32_bf16 v[244:247], v[128:131], v[216:219], v[244:247]\n\t"
        "v_mfma_f32_16x16x32_bf16 v[248:251], v[132:135], v[216:219], v[248:251]\n\t"
        "v_mfma_f32_16x16x32_bf16 v[252:255], v[136:139], v[216:219], v[252:255]\n\t"
        "v_mfma_f32_16x16x32_bf16 v[60:63], v[140:143], v[216:219], v[60:63]\n\t"
        "s_waitcnt vmcnt(7)\n\t"
        "v_mfma_f32_16x16x32_bf16 v[240:243], v[144:147], v[220:223], v[240:243]\n\t"
        "v_mfma_f32_16x16x32_bf16 v[244:247], v[148:151], v[220:223], v[244:247]\n\t"
        "v_mfma_f32_16x16x32_bf16 v[248:251], v[152:155], v[220:223], v[248:251]\n\t"
        "v_mfma_f32_16x16x32_bf16 v[252:255], v[156:159], v[220:223], v[252:255]\n\t"
        "v_mfma_f32_16x16x32_bf16 v[60:63], v[160:163], v[220:223], v[60:63]\n\t"
        "s_waitcnt vmcnt(6)\n\t"
        "v_mfma_f32_16x16x32_bf16 v[240:243], v[164:167], v[224:227], v[240:243]\n\t"
        "v_mfma_f32_16x16x32_bf16 v[244:247], v[168:171], v[224:227], v[244:247]\n\t"
        "v_mfma_f32_16x16x32_bf16 v[248:251], v[172:175], v[224:227], v[248:251]\n\t"
        "v_mfma_f32_16x16x32_bf16 v[252:255], v[176:179], v[224:227], v[252:255]\n\t"
        "v_mfma_f32_16x16x32_bf16 v[60:63], v[180:183], v[224:227], v[60:63]\n\t"
        "s_waitcnt vmcnt(5)\n\t"
        "v_mfma_f32_16x16x32_bf16 v[240:243], v[184:187], v[228:231], v[240:243]\n\t"
        "v_mfma_f32_16x16x32_bf16 v[244:247], v[188:191], v[228:231], v[244:247]\n\t"
        "v_mfma_f32_16x16x32_bf16 v[248:251], v[192:195], v[228:231], v[248:251]\n\t"
        "v_mfma_f32_16x16x32_bf16 v[252:255], v[196:199], v[228:231], v[252:255]\n\t"
        "v_mfma_f32_16x16x32_bf16 v[60:63], v[200:203], v[228:231], v[60:63]\n\t"
        "s_waitcnt vmcnt(4) lgkmcnt(10)\n\t"
        "v_mfma_f32_16x16x32_bf16 v[240:243], v[64:67], v[232:235], v[240:243]\n\t"
        "v_mfma_f32_16x16x32_bf16 v[244:247], v[68:71], v[232:235], v[244:247]\n\t"
        "v_mfma_f32_16x16x32_bf16 v[248:251], v[72:75], v[232:235], v[248:251]\n\t"
        "v_mfma_f32_16x16x32_bf16 v[252:255], v[76:79], v[232:235], v[252:255]\n\t"
        "v_mfma_f32_16x16x32_bf16 v[60:63], v[80:83], v[232:235], v[60:63]\n\t"
        "s_waitcnt vmcnt(3) lgkmcnt(0)\n\t"
        "v_mfma_f32_16x16x32_bf16 v[240:243], v[84:87], v[236:239], v[240:243]\n\t"
        "v_mfma_f32_16x16x32_bf16 v[244:247], v[88:91], v[236:239], v[244:247]\n\t"
        "v_mfma_f32_16x16x32_bf16 v[248:251], v[92:95], v[236:239], v[248:251]\n\t"
        "v_mfma_f32_16x16x32_bf16 v[252:255], v[96:99], v[236:239], v[252:255]\n\t"
        "v_mfma_f32_16x16x32_bf16 v[60:63], v[100:103], v[236:239], v[60:63]\n\t"
        "s_waitcnt vmcnt(0)\n\t"
        "s_nop 7\n\t"
            P4_OPS
            :
          "v55", "v56", "v57", "v58", "v64", "v65", "v66", "v67", "v68", "v69", "v70", "v71", "v72", "v73", "v74", "v75", "v76", "v77", "v78", "v79", "v80", "v81", "v82", "v83", "v84", "v85", "v86", "v87", "v88", "v89", "v90", "v91", "v92", "v93", "v94", "v95", "v96", "v97", "v98", "v99", "v100", "v101", "v102", "v103", "v104", "v105", "v106", "v107", "v108", "v109", "v110", "v111", "v112", "v113", "v114", "v115", "v116", "v117", "v118", "v119", "v120", "v121", "v122", "v123", "v124", "v125", "v126", "v127", "v128", "v129", "v130", "v131", "v132", "v133", "v134", "v135", "v136", "v137", "v138", "v139", "v140", "v141", "v142", "v143", "v144", "v145", "v146", "v147", "v148", "v149", "v150", "v151", "v152", "v153", "v154", "v155", "v156", "v157", "v158", "v159", "v160", "v161", "v162", "v163", "v164", "v165", "v166", "v167", "v168", "v169", "v170", "v171", "v172", "v173", "v174", "v175", "v176", "v177", "v178", "v179", "v180", "v181", "v182", "v183", "v184", "v185", "v186", "v187", "v188", "v189", "v190", "v191", "v192", "v193", "v194", "v195", "v196", "v197", "v198", "v199", "v200", "v201", "v202", "v203", "v204", "v205", "v206", "v207", "v208", "v209", "v210", "v211", "v212", "v213", "v214", "v215", "v216", "v217", "v218", "v219", "v220", "v221", "v222", "v223", "v224", "v225", "v226", "v227", "v228", "v229", "v230", "v231", "v232", "v233", "v234", "v235", "v236", "v237", "v238", "v239", "s20", "s21", "s22", "s23", "scc", "vcc", "memory"
        );
        asm volatile("" : "+v"(xv[0]), "+v"(xv[1]), "+v"(xv[2]));
        lds_st<f32x4>(red + 3 * 1024, tile(3)); lds_st<f32x4>(red + 4 * 1024, tile(4));
        BAR();
        f32x4 q[3]; s16x4 y[3];
#pragma unroll
        for (int j = 0; j < 3; ++j) { q[j] = lds_ld<f32x4>(red + j * 1024); y[j] = lds_tr(ya[j]); }
        asm volatile("" ::: "memory");
#pragma unroll
        for (int j = 0; j < 3; ++j) mix(j, tile(j) + q[j], y[j]);
    } else {
        asm volatile(
        "s_mov_b32 s21, m0\n\t"
        "ds_read_b64_tr_b16 v[64:65], %[wa] offset:0\n\t"
        "ds_read_b64_tr_b16 v[66:67], %[wa] offset:1280\n\t"
        "ds_read_b64_tr_b16 v[68:69], %[wa] offset:32\n\t"
        "ds_read_b64_tr_b16 v[70:71], %[wa] offset:1312\n\t"
        "ds_read_b64_tr_b16 v[72:73], %[wa] offset:64\n\t"
        "ds_read_b64_tr_b16 v[74:75], %[wa] offset:1344\n\t"
        "ds_read_b64_tr_b16 v[76:77], %[wa] offset:96\n\t"
        "ds_read_b64_tr_b16 v[78:79], %[wa] offset:1376\n\t"
        "ds_read_b64_tr_b16 v[80:81], %[wa] offset:128\n\t"
        "ds_read_b64_tr_b16 v[82:83], %[wa] offset:1408\n\t"
        "ds_read_b64_tr_b16 v[84:85], %[wa] offset:5760\n\t"
        "ds_read_b64_tr_b16 v[86:87], %[wa] offset:7040\n\t"
        "ds_read_b64_tr_b16 v[88:89], %[wa] offset:5792\n\t"
        "ds_read_b64_tr_b16 v[90:91], %[wa] offset:7072\n\t"
        "ds_read_b64_tr_b16 v[92:93], %[wa] offset:5824\n\t"
        "ds_read_b64_tr_b16 v[94:95], %[wa] offset:7104\n\t"
        "ds_read_b64_tr_b16 v[96:97], %[wa] offset:5856\n\t"
        "ds_read_b64_tr_b16 v[98:99], %[wa] offset:7136\n\t"
        "ds_read_b64_tr_b16 v[100:101], %[wa] offset:5888\n\t"
        "ds_read_b64_tr_b16 v[102:103], %[wa] offset:7168\n\t"
        "ds_read_b64_tr_b16 v[104:105], %[wa] offset:11520\n\t"
        "ds_read_b64_tr_b16 v[106:107], %[wa] offset:12800\n\t"
        "ds_read_b64_tr_b16 v[108:109], %[wa] offset:11552\n\t"
        "ds_read_b64_tr_b16 v[110:111], %[wa] offset:12832\n\t"
        "ds_read_b64_tr_b16 v[112:113], %[wa] offset:11584\n\t"
        "ds_read_b64_tr_b16 v[114:115], %[wa] offset:12864\n\t"
        "ds_read_b64_tr_b16 v[116:117], %[wa] offset:11616\n\t"
        "ds_read_b64_tr_b16 v[118:119], %[wa] offset:12896\n\t"
        "ds_read_b64_tr_b16 v[120:121], %[wa] offset:11648\n\t"
        "ds_read_b64_tr_b16 v[122:123], %[wa] offset:12928\n\t"
        "ds_read_b64_tr_b16 v[124:125], %[wa] offset:17280\n\t"
        "ds_read_b64_tr_b16 v[126:127], %[wa] offset:18560\n\t"
        "ds_read_b64_tr_b16 v[128:129], %[wa] offset:17312\n\t"
        "ds_read_b64_tr_b16 v[130:131], %[wa] offset:18592\n\t"
        "ds_read_b64_tr_b16 v[132:133], %[wa] offset:17344\n\t"
        "ds_read_b64_tr_b16 v[134:135], %[wa] offset:18624\n\t"
        "ds_read_b64_tr_b16 v[136:137], %[wa] offset:17376\n\t"
        "ds_read_b64_tr_b16 v[138:139], %[wa] offset:18656\n\t"
        "ds_read_b64_tr_b16 v[140:141], %[wa] offset:17408\n\t"
        "ds_read_b64_tr_b16 v[142:143], %[wa] offset:18688\n\t"
        "ds_read_b64_tr_b16 v[144:145], %[wa] offset:23040\n\t"
        "ds_read_b64_tr_b16 v[146:147], %[wa] offset:24320\n\t"
        "ds_read_b64_tr_b16 v[148:149], %[wa] offset:23072\n\t"
        "ds_read_b64_tr_b16 v[150:151], %[wa] offset:24352\n\t"
        "ds_read_b64_tr_b16 v[152:153], %[wa] offset:23104\n\t"
        "ds_read_b64_tr_b16 v[154:155], %[wa] offset:24384\n\t"
        "ds_read_b64_tr_b16 v[156:157], %[wa] offset:23136\n\t"
        "ds_read_b64_tr_b16 v[158:159], %[wa] offset:24416\n\t"
        "ds_read_b64_tr_b16 v[160:161], %[wa] offset:23168\n\t"
        "ds_read_b64_tr_b16 v[162:163], %[wa] offset:24448\n\t"
        "ds_read_b64_tr_b16 v[164:165], %[wa] offset:28800\n\t"
        "ds_read_b64_tr_b16 v[166:167], %[wa] offset:30080\n\t"
        "ds_read_b64_tr_b16 v[168:169], %[wa] offset:28832\n\t"
        "ds_read_b64_tr_b16 v[170:171], %[wa] offset:30112\n\t"
        "ds_read_b64_tr_b16 v[172:173], %[wa] offset:28864\n\t"
        "ds_read_b64_tr_b16 v[174:175], %[wa] offset:30144\n\t"
        "ds_read_b64_tr_b16 v[176:177], %[wa] offset:28896\n\t"
        "ds_read_b64_tr_b16 v[178:179], %[wa] offset:30176\n\t"
        "ds_read_b64_tr_b16 v[180:181], %[wa] offset:28928\n\t"
        "ds_read_b64_tr_b16 v[182:183], %[wa] offset:30208\n\t"
        "ds_read_b64_tr_b16 v[184:185], %[wa] offset:34560\n\t"
        "ds_read_b64_tr_b16 v[186:187], %[wa] offset:35840\n\t"
        "ds_read_b64_tr_b16 v[188:189], %[wa] offset:34592\n\t"
        "ds_read_b64_tr_b16 v[190:191], %[wa] offset:35872\n\t"
        "ds_read_b64_tr_b16 v[192:193], %[wa] offset:34624\n\t"
        "ds_read_b64_tr_b16 v[194:195], %[wa] offset:35904\n\t"
        "ds_read_b64_tr_b16 v[196:197], %[wa] offset:34656\n\t"
        "ds_read_b64_tr_b16 v[198:199], %[wa] offset:35936\n\t"
        "ds_read_b64_tr_b16 v[200:201], %[wa] offset:34688\n\t"
        "ds_read_b64_tr_b16 v[202:203], %[wa] offset:35968\n\t"
        "v_mov_b32 v240, 0\n\t"
        "v_mov_b32 v241, 0\n\t"
        "v_mov_b32 v242, 0\n\t"
        "v_mov_b32 v243, 0\n\t"
        "v_mov_b32 v244, 0\n\t"
        "v_mov_b32 v245, 0\n\t"
        "v_mov_b32 v246, 0\n\t"
        "v_mov_b32 v247, 0\n\t"
        "v_mov_b32 v248, 0\n\t"
        "v_mov_b32 v249, 0\n\t"
        "v_mov_b32 v250, 0\n\t"
        "v_mov_b32 v251, 0\n\t"
        "v_mov_b32 v252, 0\n\t"
        "v_mov_b32 v253, 0\n\t"
        "v_mov_b32 v254, 0\n\t"
        "v_mov_b32 v255, 0\n\t"
        "v_mov_b32 v60, 0\n\t"
        "v_mov_b32 v61, 0\n\t"
        "v_mov_b32 v62, 0\n\t"
        "v_mov_b32 v63, 0\n\t"
        "s_cmp_lg_u32 %[wv], 0\n\t"
        "s_cbranch_scc1 2f\n\t"
        "s_mov_b32 s20, 40000\n\t"
        "1:\n\t"
        "global_load_dwordx2 v[56:57], %[fo], %[ws] sc1\n\t"
        "s_waitcnt vmcnt(0)\n\t"
        "v_cmp_eq_u64 vcc, v[56:57], %[tag]\n\t"
        "s_cmp_eq_u64 vcc, exec\n\t"
        "s_cbranch_scc1 2f\n\t"
        "s_sleep 1\n\t"
        "s_sub_u32 s20, s20, 1\n\t"
        "s_cmp_lg_u32 s20, 0\n\t"
        "s_cbranch_scc1 1b\n\t"
        "2:\n\t"
        "s_barrier\n\t"
        "global_load_dwordx4 v[204:207], %[om], %[ws] offset:-4096 sc1\n\t"
        "global_load_dwordx4 v[208:211], %[om], %[ws] offset:-3072 sc1\n\t"
        "global_load_dwordx4 v[212:215], %[om], %[ws] offset:-2048 sc1\n\t"
        "global_load_dwordx4 v[216:219], %[om], %[ws] offset:-1024 sc1\n\t"
        "global_load_dwordx4 v[220:223], %[om], %[ws] offset:0 sc1\n\t"
        "global_load_dwordx4 v[224:227], %[om], %[ws] offset:1024 sc1\n\t"
        "global_load_dwordx4 v[228:231], %[om], %[ws] offset:2048 sc1\n\t"
        "v_add_u32 v56, 8192, %[ol]\n\t"
        "v_add_u32 v57, 16384, %[ol]\n\t"
        "v_add_u32 v58, 24576, %[ol]\n\t"
        "s_add_u32 s22, %[wsl], 131072\n\t"
        "s_addc_u32 s23, %[wsh], 0\n\t"
        "global_load_dword v232, %[ol], %[ws] sc1\n\t"
        "global_load_dword v233, v56, %[ws] sc1\n\t"
        "global_load_dword v234, v57, %[ws] sc1\n\t"
        "global_load_dword v235, v58, %[ws] sc1\n\t"
        "global_load_dword v236, %[ol], s[22:23] sc1\n\t"
        "global_load_dword v237, v56, s[22:23] sc1\n\t"
        "global_load_dword v238, v57, s[22:23] sc1\n\t"
        "global_load_dword v239, v58, s[22:23] sc1\n\t"
        "global_load_dword v59, %[go], %[ws] sc1\n\t"
        "s_mov_b32 m0, %[ym0]\n\t"
        "s_nop 0\n\t"
        "global_load_lds_dwordx4 %[yo], %[ws] sc1\n\t"
        "s_mov_b32 m0, %[ym1]\n\t"
        "s_nop 0\n\t"
        "global_load_lds_dwordx4 %[yo1], %[ws] sc1\n\t"
        "s_mov_b32 m0, s21\n\t"
        "s_waitcnt lgkmcnt(0)\n\t"
        "s_waitcnt vmcnt(17)\n\t"
        "v_mfma_f32_16x16x32_bf16 v[240:243], v[64:67], v[204:207], v[240:243]\n\t"
        "v_mfma_f32_16x16x32_bf16 v[244:247], v[68:71], v[204:207], v[244:247]\n\t"
        "v_mfma_f32_16x16x32_bf16 v[248:251], v[72:75], v[204:207], v[248:251]\n\t"
        "v_mfma_f32_16x16x32_bf16 v[252:255], v[76:79], v[204:207], v[252:255]\n\t"
        "v_mfma_f32_16x16x32_bf16 v[60:63], v[80:83], v[204:207], v[60:63]\n\t"
        "ds_read_b64_tr_b16 v[64:65], %[wl] offset:0\n\t"
        "ds_read_b64_tr_b16 v[66:67], %[wl] offset:5760\n\t"
        "ds_read_b64_tr_b16 v[68:69], %[wl] offset:32\n\t"
        "ds_read_b64_tr_b16 v[70:71], %[wl] offset:5792\n\t"
        "ds_read_b64_tr_b16 v[72:73], %[wl] offset:64\n\t"
        "ds_read_b64_tr_b16 v[74:75], %[wl] offset:5824\n\t"
        "ds_read_b64_tr_b16 v[76:77], %[wl] offset:96\n\t"
        "ds_read_b64_tr_b16 v[78:79], %[wl] offset:5856\n\t"
        "ds_read_b64_tr_b16 v[80:81], %[wl] offset:128\n\t"
        "ds_read_b64_tr_b16 v[82:83], %[wl] offset:5888\n\t"
        "s_waitcnt vmcnt(16)\n\t"
        "v_mfma_f32_16x16x32_bf16 v[240:243], v[84:87], v[208:211], v[240:243]\n\t"
        "v_mfma_f32_16x16x32_bf16 v[244:247], v[88:91], v[208:211], v[244:247]\n\t"
        "v_mfma_f32_16x16x32_bf16 v[248:251], v[92:95], v[208:211], v[248:251]\n\t"
        "v_mfma_f32_16x16x32_bf16 v[252:255], v[96:99], v[208:211], v[252:255]\n\t"
        "v_mfma_f32_16x16x32_bf16 v[60:63], v[100:103], v[208:211], v[60:63]\n\t"
        "ds_read_b64_tr_b16 v[84:85], %[wl] offset:46080\n\t"
        "ds_read_b64_tr_b16 v[86:87], %[wl] offset:51840\n\t"
        "ds_read_b64_tr_b16 v[88:89], %[wl] offset:46112\n\t"
        "ds_read_b64_tr_b16 v[90:91], %[wl] offset:51872\n\t"
        "ds_read_b64_tr_b16 v[92:93], %[wl] offset:46144\n\t"
        "ds_read_b64_tr_b16 v[94:95], %[wl] offset:51904\n\t"
        "ds_read_b64_tr_b16 v[96:97], %[wl] offset:46176\n\t"
        "ds_read_b64_tr_b16 v[98:99], %[wl] offset:51936\n\t"
        "ds_read_b64_tr_b16 v[100:101], %[wl] offset:46208\n\t"
        "ds_read_b64_tr_b16 v[102:103], %[wl] offset:51968\n\t"
        "s_waitcnt vmcnt(15)\n\t"
        "v_mfma_f32_16x16x32_bf16 v[240:243], v[104:107], v[212:215], v[240:243]\n\t"
        "v_mfma_f32_16x16x32_bf16 v[244:247], v[108:111], v[212:215], v[244:247]\n\t"
        "v_mfma_f32_16x16x32_bf16 v[248:251], v[112:115], v[212:215], v[248:251]\n\t"
        "v_mfma_f32_16x16x32_bf16 v[252:255], v[116:119], v[212:215], v[252:255]\n\t"
        "v_mfma_f32_16x16x32_bf16 v[60:63], v[120:123], v[212:215], v[60:63]\n\t"
        "s_waitcnt vmcnt(14)\n\t"
        "v_mfma_f32_16x16x32_bf16 v[240:243], v[124:127], v[216:219], v[240:243]\n\t"
        "v_mfma_f32_16x16x32_bf16 v[244:247], v[128:131], v[216:219], v[244:247]\n\t"
        "v_mfma_f32_16x16x32_bf16 v[248:251], v[132:135], v[216:219], v[248:251]\n\t"
        "v_mfma_f32_16x16x32_bf16 v[252:255], v[136:139], v[216:219], v[252:255]\n\t"
        "v_mfma_f32_16x16x32_bf16 v[60:63], v[140:143], v[216:219], v[60:63]\n\t"
        "s_waitcnt vmcnt(13)\n\t"
        "v_mfma_f32_16x16x32_bf16 v[240:243], v[144:147], v[220:223], v[240:243]\n\t"
        "v_mfma_f32_16x16x32_bf16 v[244:247], v[148:151], v[220:223], v[244:247]\n\t"
        "v_mfma_f32_16x16x32_bf16 v[248:251], v[152:155], v[220:223], v[248:251]\n\t"
        "v_mfma_f32_16x16x32_bf16 v[252:255], v[156:159], v[220:223], v[252:255]\n\t"
        "v_mfma_f32_16x16x32_bf16 v[60:63], v[160:163], v[220:223], v[60:63]\n\t"
        "s_waitcnt vmcnt(12)\n\t"
        "v_mfma_f32_16x16x32_bf16 v[240:243], v[164:167], v[224:227], v[240:243]\n\t"
        "v_mfma_f32_16x16x32_bf16 v[244:247], v[168:171], v[224:227], v[244:247]\n\t"
        "v_mfma_f32_16x16x32_bf16 v[248:251], v[172:175], v[224:227], v[248:251]\n\t"
        "v_mfma_f32_16x16x32_bf16 v[252:255], v[176:179], v[224:227], v[252:255]\n\t"
        "v_mfma_f32_16x16x32_bf16 v[60:63], v[180:183], v[224:227], v[60:63]\n\t"
        "s_waitcnt vmcnt(11)\n\t"
        "v_mfma_f32_16x16x32_bf16 v[240:243], v[184:187], v[228:231], v[240:243]\n\t"
        "v_mfma_f32_16x16x32_bf16 v[244:247], v[188:191], v[228:231], v[244:247]\n\t"
        "v_mfma_f32_16x16x32_bf16 v[248:251], v[192:195], v[228:231], v[248:251]\n\t"
        "v_mfma_f32_16x16x32_bf16 v[252:255], v[196:199], v[228:231], v[252:255]\n\t"
        "v_mfma_f32_16x16x32_bf16 v[60:63], v[200:203], v[228:231], v[60:63]\n\t"
        "s_waitcnt vmcnt(7) lgkmcnt(10)\n\t"
        "v_mfma_f32_16x16x32_bf16 v[240:243], v[64:67], v[232:235], v[240:243]\n\t"
        "v_mfma_f32_16x16x32_bf16 v[244:247], v[68:71], v[232:235], v[244:247]\n\t"
        "v_mfma_f32_16x16x32_bf16 v[248:251], v[72:75], v[232:235], v[248:251]\n\t"
        "v_mfma_f32_16x16x32_bf16 v[252:255], v[76:79], v[232:235], v[252:255]\n\t"
        "v_mfma_f32_16x16x32_bf16 v[60:63], v[80:83], v[232:235], v[60:63]\n\t"
        "s_waitcnt vmcnt(3) lgkmcnt(0)\n\t"
        "v_mfma_f32_16x16x32_bf16 v[240:243], v[84:87], v[236:239], v[240:243]\n\t"
        "v_mfma_f32_16x16x32_bf16 v[244:247], v[88:91], v[236:239], v[244:247]\n\t"
        "v_mfma_f32_16x16x32_bf16 v[248:251], v[92:95], v[236:239], v[248:251]\n\t"
        "v_mfma_f32_16x16x32_bf16 v[252:255], v[96:99], v[236:239], v[252:255]\n\t"
        "v_mfma_f32_16x16x32_bf16 v[60:63], v[100:103], v[236:239], v[60:63]\n\t"
        "s_waitcnt vmcnt(0)\n\t"
        "s_nop 7\n\t"
            P4_OPS
            :
          "v55", "v56", "v57", "v58", "v64", "v65", "v66", "v67", "v68", "v69", "v70", "v71", "v72", "v73", "v74", "v75", "v76", "v77", "v78", "v79", "v80", "v81", "v82", "v83", "v84", "v85", "v86", "v87", "v88", "v89", "v90", "v91", "v92", "v93", "v94", "v95", "v96", "v97", "v98", "v99", "v100", "v101", "v102", "v103", "v104", "v105", "v106", "v107", "v108", "v109", "v110", "v111", "v112", "v113", "v114", "v115", "v116", "v117", "v118", "v119", "v120", "v121", "v122", "v123", "v124", "v125", "v126", "v127", "v128", "v129", "v130", "v131", "v132", "v133", "v134", "v135", "v136", "v137", "v138", "v139", "v140", "v141", "v142", "v143", "v144", "v145", "v146", "v147", "v148", "v149", "v150", "v151", "v152", "v153", "v154", "v155", "v156", "v157", "v158", "v159", "v160", "v161", "v162", "v163", "v164", "v165", "v166", "v167", "v168", "v169", "v170", "v171", "v172", "v173", "v174", "v175", "v176", "v177", "v178", "v179", "v180", "v181", "v182", "v183", "v184", "v185", "v186", "v187", "v188", "v189", "v190", "v191", "v192", "v193", "v194", "v195", "v196", "v197", "v198", "v199", "v200", "v201", "v202", "v203", "v204", "v205", "v206", "v207", "v208", "v209", "v210", "v211", "v212", "v213", "v214", "v215", "v216", "v217", "v218", "v219", "v220", "v221", "v222", "v223", "v224", "v225", "v226", "v227", "v228", "v229", "v230", "v231", "v232", "v233", "v234", "v235", "v236", "v237", "v238", "v239", "s20", "s21", "s22", "s23", "scc", "vcc", "memory"
        );
        asm volatile("" : "+v"(xv[0]), "+v"(xv[1]), "+v"(xv[2]));
        lds_st<f32x4>(red, tile(0)); lds_st<f32x4>(red + 1024, tile(1)); lds_st<f32x4>(red + 2048, tile(2));
        BAR();
        f32x4 q[2]; s16x4 y[2];
#pragma unroll
        for (int j = 0; j < 2; ++j) { q[j] = lds_ld<f32x4>(red + (3 + j) * 1024); y[j] = lds_tr(ya[j]); }
        asm volatile("" ::: "memory");
#pragma unroll
        for (int j = 0; j < 2; ++j) mix(j, tile(3 + j) + q[j], y[j]);
    }
}

constexpr int LDS_BYTES = 163840;

__global__ __launch_bounds__(512, 2) void k_fused(Ptrs p) {
    __shared__ __attribute__((aligned(16))) char lds_[LDS_BYTES];
    const u32 lds = ldsaddr(lds_);
    const int b = blockIdx.x;
    const rsrc_t ws = __builtin_amdgcn_make_buffer_rsrc(p.ws, 0, WS_END, 0x00020000);
    const char* fb = uni64(p.ws + WS_F);
    asm volatile("" :: "s"(p.x), "s"(p.wqkv), "s"(p.wo), "s"(p.bk), "s"(p.bv), "s"(p.shk), "s"(p.ssmk), "s"(p.ssmd), "s"(p.gw), "s"(p.out), "s"(p.ws));
    const Carry cy = phase_qkv(p, ws, b, lds);
    const u64 tag = cy.tag;
    AttnSetup as;
    phase_conv(p, ws, b, lds, fb, cy, as);
    phase_attn(p, ws, b, lds, tag, as);
    phase_out(p, ws, b, lds, tag);
}

extern "C" void kernel_launch(void* const* d_in, const int* in_sizes, int n_in, void* d_out, int out_size, void* d_ws, size_t ws_size,
                              hipStream_t stream) {
    Ptrs p;
    p.x = (const u16*)d_in[0]; p.wqkv = (const u16*)d_in[1]; p.wo = (const u16*)d_in[2];
    p.bk = (const u16*)d_in[4]; p.bv = (const u16*)d_in[5]; p.shk = (const u16*)d_in[6];
    p.ssmk = (const u16*)d_in[7]; p.ssmd = (const u16*)d_in[8]; p.gw = (const u16*)d_in[9];
    p.out = (u16*)d_out; p.ws = (char*)d_ws;
    k_fused<<<256, 512, 0, stream>>>(p);
}
```
